```python
import torch

def _softmax(x, axis=-1):
    return torch.softmax(x, dim=axis)

def _gelu(x):
    return torch.nn.functional.gelu(x, approximate='tanh')

def _silu(x):
    return torch.nn.functional.silu(x)

def _sigmoid(x):
    return torch.sigmoid(x)

def _layernorm(x, axis=-1, eps=1e-05):
    m = x.mean(dim=axis, keepdim=True)
    v = x.var(dim=axis, keepdim=True, unbiased=False)
    return (x - m) / torch.sqrt(v + eps)

def _scatter_add(tgt, idx, src, axis=0):
    return tgt.clone().index_add_(axis, idx, src)

def _segment_sum(src, seg, n):
    out = torch.zeros((n,) + src.shape[1:], dtype=src.dtype)
    return out.index_add_(0, seg, src)

def _pad(x, pw):
    flat = tuple((v for pair in reversed(pw) for v in pair))
    return torch.nn.functional.pad(x, flat)
kernel_name = 'parallel_sum__memory_aug_recurrent_mem_tr__glu_gated_geglu__prompt'

def setup_inputs():

    _w = lambda a: (lambda t: t.to(torch.bfloat16) if t.is_floating_point() else t)(torch.as_tensor(a))

    def _A_setup():
        B, S, Dm = (1, 2048, 576)
        H, Dh = (32, 128)
        M, seg = (8, 32)
        seg = min(seg, S)
        S = S // seg * seg
        return {'x': _w((lambda *s: torch.randn(*s, dtype=torch.bfloat16) * 0.05)(B, S, Dm)), 'mem0': _w((lambda *s: torch.randn(*s, dtype=torch.bfloat16) * 0.2)(B, M, Dm)), 'Wq': _w((lambda *s: torch.randn(*s, dtype=torch.bfloat16) * 1.6)(Dm, H, Dh) * Dm ** (-0.5)), 'Wk': _w((lambda *s: torch.randn(*s, dtype=torch.bfloat16) * 1.6)(Dm, H, Dh) * Dm ** (-0.5)), 'Wv': _w((lambda *s: torch.randn(*s, dtype=torch.bfloat16) * 0.2)(Dm, H, Dh) * Dm ** (-0.5)), 'Wo': _w((lambda *s: torch.randn(*s, dtype=torch.bfloat16) * 0.2)(H, Dh, Dm) * (H * Dh) ** (-0.5)), 'seg_len': seg}
    _A_in = _A_setup()

    def _B_setup():
        B, S, Dm, Df = (1, 2048, 576, 4608)
        return {'x': _w((lambda *s: torch.randn(*s, dtype=torch.bfloat16) * 0.05)(B, S, Dm)), 'w_gate': _w((lambda *s: torch.randn(*s, dtype=torch.bfloat16) * 0.4)(Dm, Df) * Dm ** (-0.5)), 'w_up': _w((lambda *s: torch.randn(*s, dtype=torch.bfloat16) * 0.4)(Dm, Df) * Dm ** (-0.5)), 'w_down': _w((lambda *s: torch.randn(*s, dtype=torch.bfloat16) * 0.4)(Df, Dm) * Df ** (-0.5))}
    _B_in = _B_setup()
    return {'A_x': _A_in['x'], 'A_mem0': _A_in['mem0'], 'A_Wq': _A_in['Wq'], 'A_Wk': _A_in['Wk'], 'A_Wv': _A_in['Wv'], 'A_Wo': _A_in['Wo'], 'A_seg_len': _A_in['seg_len'], 'B_x': _B_in['x'], 'B_w_gate': _B_in['w_gate'], 'B_w_up': _B_in['w_up'], 'B_w_down': _B_in['w_down']}

def reference(A_x, A_mem0, A_Wq, A_Wk, A_Wv, A_Wo, A_seg_len, B_x, B_w_gate, B_w_up, B_w_down):

    def _A_ref(x, mem0, Wq, Wk, Wv, Wo, seg_len):
        B, S, Dm = x.shape
        H, Dh = (Wq.shape[1], Wq.shape[2])
        M = mem0.shape[1]
        scale = Dh ** (-0.5)
        n_seg = S // seg_len
        L = M + seg_len
        idx = torch.arange(L)
        mask = (idx[:, None] < M) | (idx[:, None] >= idx[None, :])
        mem = mem0
        outs = []
        for s in range(n_seg):
            xs = x[:, s * seg_len:(s + 1) * seg_len, :].clone()
            h = torch.concatenate([mem, xs], axis=1)
            q = torch.einsum('btd,dhe->bhte', h, Wq)
            k = torch.einsum('btd,dhe->bhte', h, Wk)
            v = torch.einsum('btd,dhe->bhte', h, Wv)
            scores = torch.einsum('bhqe,bhke->bhqk', q, k) * scale
            scores = torch.where(mask[None, None], scores, -1000000000.0)
            p = _softmax(scores, axis=-1)
            o = torch.einsum('bhqk,bhke->bhqe', p, v)
            o = torch.einsum('bhte,hed->btd', o, Wo)
            mem = o[:, :M, :].clone()
            outs.append(o[:, M:, :])
        return torch.concatenate(outs, axis=1)

    def _B_ref(x, w_gate, w_up, w_down):
        g = torch.einsum('bsd,df->bsf', x, w_gate)
        u = torch.einsum('bsd,df->bsf', x, w_up)
        h = _gelu(g) * u
        return torch.einsum('bsf,fd->bsd', h, w_down)
    x = A_x
    xn = _layernorm(x, axis=-1)
    return x + _A_ref(xn, A_mem0, A_Wq, A_Wk, A_Wv, A_Wo, A_seg_len) + _B_ref(xn, B_w_gate, B_w_up, B_w_down)
```

```cpp
#include <hip/hip_runtime.h>
#include <stdint.h>
#pragma clang diagnostic ignored "-Winline-asm"
#define LAS __attribute__((address_space(3)))
#define DI __device__ __forceinline__
typedef unsigned short u16;
typedef __bf16 bf2 __attribute__((ext_vector_type(2)));
typedef __bf16 bf8 __attribute__((ext_vector_type(8)));
typedef __bf16 bf16x16 __attribute__((ext_vector_type(16)));
typedef short s4 __attribute__((ext_vector_type(4)));
typedef short s8 __attribute__((ext_vector_type(8)));
typedef short s16v __attribute__((ext_vector_type(16)));
typedef unsigned short us2 __attribute__((ext_vector_type(2)));
typedef float f2 __attribute__((ext_vector_type(2)));
typedef float f4 __attribute__((ext_vector_type(4)));
typedef unsigned u2 __attribute__((ext_vector_type(2)));
typedef unsigned u4 __attribute__((ext_vector_type(4)));
typedef __amdgpu_buffer_rsrc_t rsrc_t;

constexpr int S_ = 2048, D_ = 576, H_ = 32, F_ = 4608, HE_ = 4096;
constexpr unsigned WS_BYTES = 254u << 20;
#define NR_RS 1
constexpr unsigned OFF_RS = 0;
constexpr unsigned RS_SLOT = 36 * 16384;
constexpr unsigned OFF_AG = 2 * RS_SLOT;
constexpr unsigned AG_SLOT = 9 * 16384;
constexpr int NR_AG = 2;
constexpr unsigned OFF_TB = OFF_AG + 4 * AG_SLOT;
constexpr unsigned OFF_PRB = OFF_TB + 16384;
constexpr unsigned OFF_FKV = OFF_PRB + 16384;
constexpr unsigned OFF_FQ = OFF_FKV + 65536;
constexpr unsigned OFF_FD = OFF_FQ + 32768;
constexpr unsigned OFF_FM = OFF_FD + 16384 + 4096;
constexpr unsigned OFF_FP = OFF_FM + 64 * 64 * 64;
constexpr unsigned CLR_END = OFF_FP + 64 * 64 * 64;
constexpr unsigned OFF_SEL = CLR_END;
constexpr unsigned OFF_GEN = OFF_SEL + 64;
constexpr unsigned OFF_FX = OFF_SEL + 4096;
static_assert(OFF_FKV <= (2u << 20), "page");
constexpr unsigned OFF_XN = OFF_FX + 12288;
constexpr unsigned OFF_KX = OFF_XN + S_ * D_ * 2;
constexpr unsigned OFF_VX = OFF_KX + S_ * HE_ * 2;
constexpr unsigned OFF_QX = OFF_VX + S_ * HE_ * 2;
constexpr unsigned OFF_KM = OFF_QX + S_ * HE_ * 2;
constexpr unsigned OFF_VM = OFF_KM + 64 * 32 * 2048;
constexpr unsigned OFF_PART = OFF_VM + 64 * 32 * 4096;
constexpr unsigned PART_SLOT = 2 * 18 * 1024;
constexpr int NPART = 24;
constexpr unsigned OFF_DBG = OFF_PART + 64 * NPART * PART_SLOT;
static_assert(OFF_DBG + (5u << 20) <= WS_BYTES, "ws");

DI rsrc_t mk_rsrc(const void* p, unsigned bytes) { return __builtin_amdgcn_make_buffer_rsrc((void*)p, (short)0, (int)bytes, 0x00020000); }
DI void SB() { __builtin_amdgcn_sched_barrier(0); }
template <int N> DI void wvm() { asm volatile("s_waitcnt vmcnt(%0)" ::"n"(N) : "memory"); }
DI void bar() { asm volatile("s_waitcnt lgkmcnt(0)\n\ts_barrier" ::: "memory"); }
DI void bar_nowait() { asm volatile("s_barrier" ::: "memory"); }
DI void dma(unsigned ldsaddr, unsigned voff, rsrc_t r, unsigned soff) {
  asm volatile("s_mov_b32 m0, %0\n\ts_nop 0\n\tbuffer_load_dwordx4 %1, %2, %3 offen lds" ::"s"(ldsaddr), "v"(voff), "s"(r), "s"(soff) : "memory");
}
DI void dma_sc1(unsigned ldsaddr, unsigned voff, rsrc_t r, unsigned soff) {
  asm volatile("s_mov_b32 m0, %0\n\ts_nop 0\n\tbuffer_load_dwordx4 %1, %2, %3 offen sc1 lds" ::"s"(ldsaddr), "v"(voff), "s"(r), "s"(soff) : "memory");
}
DI void dma_i(unsigned ldsbase, unsigned ldsimm, unsigned voff, rsrc_t r, unsigned soffimm) {
  unsigned t;
  asm volatile("s_add_u32 m0, %1, %2\n\ts_mov_b32 %0, %5\n\tbuffer_load_dwordx4 %3, %4, %0 offen lds" : "=&s"(t) : "s"(ldsbase), "n"(ldsimm), "v"(voff), "s"(r), "n"(soffimm) : "memory", "scc");
}
DI void dma_sc1_i(unsigned ldsbase, unsigned voff, rsrc_t r, unsigned soff, unsigned imm) {
  unsigned t;
  asm volatile("s_add_u32 m0, %1, %5\n\ts_add_u32 %0, %4, %5\n\tbuffer_load_dwordx4 %2, %3, %0 offen sc1 lds" : "=&s"(t) : "s"(ldsbase), "v"(voff), "s"(r), "s"(soff), "n"(imm) : "memory", "scc");
}
constexpr int AUX_SC1 = 16;
DI u4 ld16(rsrc_t r, unsigned voff, unsigned soff) { return __builtin_amdgcn_raw_buffer_load_b128(r, voff, soff, AUX_SC1); }
DI u2 ld8(rsrc_t r, unsigned voff, unsigned soff) { return __builtin_amdgcn_raw_buffer_load_b64(r, voff, soff, AUX_SC1); }
DI unsigned ld4(rsrc_t r, unsigned voff, unsigned soff) { return __builtin_amdgcn_raw_buffer_load_b32(r, voff, soff, AUX_SC1); }
DI u4 ld16p(rsrc_t r, unsigned voff, unsigned soff) { return __builtin_amdgcn_raw_buffer_load_b128(r, voff, soff, 0); }
DI void st16(u4 v, rsrc_t r, unsigned voff, unsigned soff) { __builtin_amdgcn_raw_buffer_store_b128(v, r, voff, soff, AUX_SC1); }
DI void st8(u2 v, rsrc_t r, unsigned voff, unsigned soff) { __builtin_amdgcn_raw_buffer_store_b64(v, r, voff, soff, AUX_SC1); }
DI void st4(unsigned v, rsrc_t r, unsigned voff, unsigned soff) { __builtin_amdgcn_raw_buffer_store_b32(v, r, voff, soff, AUX_SC1); }
DI unsigned pk2(float a, float b) { f2 v = {a, b}; return __builtin_bit_cast(unsigned, __builtin_convertvector(v, bf2)); }
DI float asf(unsigned u) { return __builtin_bit_cast(float, u); }
DI unsigned asu(float f) { return __builtin_bit_cast(unsigned, f); }
DI float bf2f(u16 v) { return asf(((unsigned)v) << 16); }
DI float rbf(float f) { return (float)(__bf16)f; }
DI unsigned pkmaxu16(unsigned a, unsigned b) { return __builtin_bit_cast(unsigned, __builtin_elementwise_max(__builtin_bit_cast(us2, a), __builtin_bit_cast(us2, b))); }
DI unsigned pkminu16(unsigned a, unsigned b) { return __builtin_bit_cast(unsigned, __builtin_elementwise_min(__builtin_bit_cast(us2, a), __builtin_bit_cast(us2, b))); }
DI s4 trd(LAS char* base, unsigned off) { return __builtin_amdgcn_ds_read_tr16_b64_v4i16((LAS s4*)(base + off)); }
DI bf8 cat8(s4 a, s4 b) { return __builtin_bit_cast(bf8, __builtin_shufflevector(a, b, 0, 1, 2, 3, 4, 5, 6, 7)); }
DI bf16x16 cat16(s4 a, s4 b, s4 c, s4 d) {
  s8 x = __builtin_shufflevector(a, b, 0, 1, 2, 3, 4, 5, 6, 7), y = __builtin_shufflevector(c, d, 0, 1, 2, 3, 4, 5, 6, 7);
  return __builtin_bit_cast(bf16x16, __builtin_shufflevector(x, y, 0, 1, 2, 3, 4, 5, 6, 7, 8, 9, 10, 11, 12, 13, 14, 15));
}
DI f4 smf(bf8 a, bf16x16 b, f4 c, int idx) { return __builtin_amdgcn_smfmac_f32_16x16x64_bf16(a, b, c, idx, 0, 0); }
DI f4 mf32(bf8 a, bf8 b, f4 c) { return __builtin_amdgcn_mfma_f32_16x16x32_bf16(a, b, c, 0, 0, 0); }
DI f4 mf16(u2 a, u2 b, f4 c) { return __builtin_amdgcn_mfma_f32_16x16x16bf16_1k(__builtin_bit_cast(s4, a), __builtin_bit_cast(s4, b), c, 0, 0, 0); }
DI float swap32_add(float a, float b) { auto r = __builtin_amdgcn_permlane32_swap(asu(a), asu(b), false, false); return asf(r[0]) + asf(r[1]); }
DI float xmax16(float a) { auto r = __builtin_amdgcn_permlane16_swap(asu(a), asu(a), false, false); return fmaxf(asf(r[0]), asf(r[1])); }
DI float xmax32(float a) { auto r = __builtin_amdgcn_permlane32_swap(asu(a), asu(a), false, false); return fmaxf(asf(r[0]), asf(r[1])); }
DI float xadd16(float a) { auto r = __builtin_amdgcn_permlane16_swap(asu(a), asu(a), false, false); return asf(r[0]) + asf(r[1]); }
DI float xadd32(float a) { auto r = __builtin_amdgcn_permlane32_swap(asu(a), asu(a), false, false); return asf(r[0]) + asf(r[1]); }
DI void delay(int n) { for (int i = 0; i < n; ++i) asm volatile("s_nop 3"); }
constexpr float SCL2 = 0.08838834764831845f * 1.4426950408889634f;
#define SPINMAX (1 << 17)
constexpr int SPIN_MAX = SPINMAX;

DI u2 tag_of(u2 g) { return u2{(unsigned)__builtin_amdgcn_readfirstlane(g.x) + 1u, (unsigned)__builtin_amdgcn_readfirstlane(g.y)}; }
DI u2 call_tag(rsrc_t r) { return tag_of(ld8(r, 0, OFF_GEN)); }
DI bool tagged(u4 v, u2 tag) { return __ballot(v.x != tag.x || v.y != tag.y || v.z != tag.x || v.w != tag.y) == 0ull; }
DI void wait_fx(rsrc_t r, unsigned voff, u2 tag) {
  for (int sp = 0; sp < SPIN_MAX; ++sp) {
    if (tagged(ld16(r, voff, OFF_FX), tag)) break;
    asm volatile("s_sleep 1" ::: "memory");
  }
  asm volatile("" ::: "memory");
}

#define WST(id, i)
#define WSTL(id, i)
#define WST1(id, i)

#define R_LOOP_ASM \
  "s_mov_b32 s86, s65\n" \
  "s_mov_b32 s87, s66\n" \
  "s_add_u32 s88, s86, 16\n" \
  "s_lshl_b32 s89, s88, 1\n" \
  "s_mov_b32 s65, 0\n" \
  "s_mov_b32 s66, 0\n" \
  "s_mov_b32 s67, 589824\n" \
  "s_mov_b32 s74, 0xff00ff00\n" \
  "s_mov_b32 s75, 0xff00ff00\n" \
  "s_mov_b32 s83, 0\n" \
  "s_mov_b32 s84, 0\n" \
  "s_mov_b32 s77, 0\n" \
  "s_branch L_KA%=\n" \
  "L_KAB%=:\n" \
  "s_mov_b32 m0, s50\n" \
  "s_nop 0\n" \
  "buffer_load_dwordx4 v0, s[36:39], s49 offen offset:0 sc1 lds\n" \
  "buffer_load_dwordx4 v0, s[36:39], s49 offen offset:1024 sc1 lds\n" \
  "buffer_load_dwordx4 v0, s[36:39], s49 offen offset:2048 sc1 lds\n" \
  "buffer_load_dwordx4 v0, s[36:39], s49 offen offset:3072 sc1 lds\n" \
  "s_and_b32 s85, s50, 0xc000\n" \
  "s_xor_b32 s50, s50, 0xc000\n" \
  "s_add_u32 s49, s49, 0x40000\n" \
  "s_memrealtime s[70:71]\n" \
  "s_waitcnt lgkmcnt(0)\n" \
  "s_mov_b32 s68, s70\n" \
  "L_PRE%=:\n" \
  "s_add_u32 s77, s65, 1\n" \
  "s_min_u32 s77, s77, 63\n" \
  "s_bitcmp1_b32 s59, 4\n" \
  "s_cselect_b32 s77, s77, 0\n" \
  "s_cmp_gt_u32 s66, s77\n" \
  "s_cbranch_scc0 L_KV%=\n" \
  "L_KVB%=:\n" \
  "v_and_b32 v202, 0x70, v0\n" \
  "ds_read_b128 v[226:229], v202 offset:11520\n" \
  "ds_read_b128 v[146:149], v238 offset:0\n" \
  "ds_read_b128 v[150:153], v238 offset:1024\n" \
  "ds_read_b128 v[154:157], v238 offset:2048\n" \
  "ds_read_b128 v[158:161], v238 offset:3072\n" \
  "ds_read_b128 v[162:165], v238 offset:16384\n" \
  "ds_read_b128 v[166:169], v238 offset:17408\n" \
  "ds_read_b128 v[170:173], v238 offset:18432\n" \
  "ds_read_b128 v[174:177], v238 offset:19456\n" \
  "ds_read_b128 v[178:181], v238 offset:32768\n" \
  "ds_read_b128 v[182:185], v238 offset:33792\n" \
  "ds_read_b128 v[186:189], v238 offset:34816\n" \
  "ds_read_b128 v[190:193], v238 offset:35840\n" \
  "v_lshrrev_b32 v203, 7, v0\n" \
  "v_lshl_or_b32 v202, v203, s63, v202\n" \
  "v_mov_b64 v[230:231], 0\n" \
  "v_mov_b64 v[232:233], 0\n" \
  "v_mov_b64 v[234:235], 0\n" \
  "v_mov_b64 v[236:237], 0\n" \
  "s_and_b32 s78, s65, 3\n" \
  "s_mul_i32 s82, s78, 147456\n" \
  "s_xor_b32 s78, s78, 2\n" \
  "s_mul_i32 s78, s78, 147456\n" \
  "s_add_u32 s78, s78, s46\n" \
  "s_add_u32 s79, s47, s67\n" \
  "s_add_u32 s80, s46, s82\n" \
  "s_add_u32 s81, s44, s82\n" \
  "s_add_u32 s82, s45, s82\n" \
  "s_bitcmp1_b32 s59, 3\n" \
  "s_cbranch_scc0 L_POLL%=\n" \
  "s_mov_b32 s83, 0\n" \
  "s_waitcnt lgkmcnt(12)\n" \
  "s_waitcnt vmcnt(4)\n" \
  "v_mfma_f32_16x16x32_bf16 v[206:209], v[210:213], v[226:229], 0\n" \
  "v_max3_u32 v194, v210, v211, v212\n" \
  "v_max_u32 v194, v194, v213\n" \
  "s_waitcnt vmcnt(3)\n" \
  "v_mfma_f32_16x16x32_bf16 v[206:209], v[214:217], v[226:229], v[206:209]\n" \
  "v_max3_u32 v194, v194, v214, v215\n" \
  "v_max3_u32 v194, v194, v216, v217\n" \
  "s_waitcnt vmcnt(2)\n" \
  "v_mfma_f32_16x16x32_bf16 v[206:209], v[218:221], v[226:229], v[206:209]\n" \
  "v_max3_u32 v194, v194, v218, v219\n" \
  "v_max3_u32 v194, v194, v220, v221\n" \
  "s_waitcnt vmcnt(1)\n" \
  "v_mfma_f32_16x16x32_bf16 v[206:209], v[222:225], v[226:229], v[206:209]\n" \
  "v_max3_u32 v194, v194, v222, v223\n" \
  "v_max3_u32 v194, v194, v224, v225\n" \
  "v_cmp_eq_u32 vcc, -1, v194\n" \
  "s_cbranch_vccnz L_RSR%=\n" \
  "L_RSOK%=:\n" \
  "s_nop 3\n" \
  "v_permlane32_swap_b32 v206, v208\n" \
  "v_permlane32_swap_b32 v207, v209\n" \
  "v_pk_add_f32 v[206:207], v[206:207], v[208:209]\n" \
  "v_cvt_pk_bf16_f32 v208, v206, v207\n" \
  "v_cvt_pk_bf16_f32 v209, v206, v207\n" \
  "s_nop 1\n" \
  "v_permlane32_swap_b32 v208, v209\n" \
  "v_mov_b64 v[206:207], v[208:209]\n" \
  "s_nop 1\n" \
  "v_permlane16_swap_b32 v206, v208\n" \
  "v_permlane16_swap_b32 v207, v209\n" \
  "buffer_store_dwordx4 v[206:209], v202, s[36:39], s80 offen sc1\n" \
  "v_mov_b64 v[212:213], -1\n" \
  "v_mov_b64 v[214:215], -1\n" \
  "buffer_store_dwordx4 v[212:215], v0, s[36:39], s79 offen offset:0 sc1\n" \
  "buffer_store_dwordx4 v[212:215], v0, s[36:39], s79 offen offset:1024 sc1\n" \
  "buffer_store_dwordx4 v[212:215], v0, s[36:39], s79 offen offset:2048 sc1\n" \
  "buffer_store_dwordx4 v[212:215], v0, s[36:39], s79 offen offset:3072 sc1\n" \
  "buffer_store_dwordx4 v[212:215], v202, s[36:39], s78 offen sc1\n" \
  "L_POLL%=:\n" \
  "s_mov_b32 s80, 0\n" \
  "s_xor_b32 s67, s67, 589824\n" \
  "s_and_b32 s78, s65, 1\n" \
  "s_lshl_b32 s78, s78, 12\n" \
  "s_add_u32 s79, s54, s78\n" \
  "s_add_u32 s78, s48, s67\n" \
  "s_bitcmp1_b32 s59, 4\n" \
  "s_barrier\n" \
  "s_cbranch_scc0 L_BAR%=\n" \
  "s_memrealtime s[72:73]\n" \
  "s_mov_b32 m0, s60\n" \
  "s_waitcnt lgkmcnt(0)\n" \
  "s_add_u32 s76, s72, 4\n" \
  "s_sub_u32 s77, s76, s68\n" \
  "s_cmp_gt_i32 s77, 0\n" \
  "s_cselect_b32 s68, s76, s68\n" \
  "s_mov_b32 s76, 0\n" \
  "L_U2%=:\n" \
  "s_memrealtime s[72:73]\n" \
  "s_add_u32 s76, s76, 0x400000\n" \
  "s_cbranch_scc1 L_U2%=x\n" \
  "s_waitcnt lgkmcnt(0)\n" \
  "s_sub_u32 s77, s72, s68\n" \
  "s_cmp_lt_i32 s77, 0\n" \
  "s_cbranch_scc1 L_U2%=\n" \
  "L_U2%=x:\n" \
  "buffer_load_dwordx4 v0, s[36:39], s81 offen offset:0 sc1 lds\n" \
  "buffer_load_dwordx4 v0, s[36:39], s81 offen offset:1024 sc1 lds\n" \
  "buffer_load_dwordx4 v0, s[36:39], s81 offen offset:2048 sc1 lds\n" \
  "buffer_load_dwordx4 v0, s[36:39], s81 offen offset:3072 sc1 lds\n" \
  "s_mov_b32 m0, s61\n" \
  "s_mov_b32 exec_hi, 0\n" \
  "buffer_load_dwordx4 v0, s[36:39], s82 offen sc1 lds\n" \
  "s_mov_b32 exec_hi, -1\n" \
  "s_waitcnt vmcnt(0)\n" \
  "L_BAR%=:\n" \
  "s_waitcnt lgkmcnt(0)\n" \
  "s_barrier\n" \
  "L_STEP%=:\n" \
  "s_memrealtime s[70:71]\n" \
  "ds_read_b128 v[194:197], v0 offset:0\n" \
  "ds_read_b128 v[198:201], v0 offset:1024\n" \
  "ds_read_b128 v[202:205], v0 offset:2048\n" \
  "ds_read_b128 v[206:209], v0 offset:3072\n" \
  "s_mov_b32 m0, s50\n" \
  "s_waitcnt lgkmcnt(3)\n" \
  "v_smfmac_f32_16x16x64_bf16 v[230:233], v[194:197], v[146:153], v246\n" \
  "ds_read_b128 v[210:213], v0 offset:4096\n" \
  "ds_read_b128 v[214:217], v0 offset:5120\n" \
  "v_smfmac_f32_16x16x64_bf16 v[234:237], v[194:197], v[154:161], v246\n" \
  "ds_read_b128 v[218:221], v0 offset:6144\n" \
  "s_waitcnt lgkmcnt(5)\n" \
  "v_smfmac_f32_16x16x64_bf16 v[230:233], v[198:201], v[162:169], v246\n" \
  "ds_read_b128 v[222:225], v0 offset:7168\n" \
  "ds_read_b128 v[226:229], v0 offset:8192\n" \
  "v_smfmac_f32_16x16x64_bf16 v[234:237], v[198:201], v[170:177], v246\n" \
  "ds_read_b128 v[146:149], v238 offset:49152\n" \
  "s_waitcnt lgkmcnt(7)\n" \
  "v_smfmac_f32_16x16x64_bf16 v[230:233], v[202:205], v[178:185], v246\n" \
  "ds_read_b128 v[150:153], v238 offset:50176\n" \
  "ds_read_b128 v[154:157], v238 offset:51200\n" \
  "v_smfmac_f32_16x16x64_bf16 v[234:237], v[202:205], v[186:193], v246\n" \
  "ds_read_b128 v[158:161], v238 offset:52224\n" \
  "s_waitcnt lgkmcnt(9)\n" \
  "v_smfmac_f32_16x16x64_bf16 v[230:233], v[206:209], v[82:89], v246\n" \
  "ds_read_b128 v[162:165], v239 offset:0\n" \
  "ds_read_b128 v[166:169], v239 offset:1024\n" \
  "v_smfmac_f32_16x16x64_bf16 v[234:237], v[206:209], v[90:97], v246\n" \
  "ds_read_b128 v[170:173], v239 offset:2048\n" \
  "s_waitcnt lgkmcnt(10)\n" \
  "v_smfmac_f32_16x16x64_bf16 v[230:233], v[210:213], v[98:105], v246\n" \
  "ds_read_b128 v[174:177], v239 offset:3072\n" \
  "v_mov_b64 v[178:179], 0\n" \
  "v_smfmac_f32_16x16x64_bf16 v[234:237], v[210:213], v[106:113], v246\n" \
  "buffer_load_dwordx4 v0, s[36:39], s49 offen offset:0 sc1 lds\n" \
  "v_mov_b64 v[180:181], 0\n" \
  "v_smfmac_f32_16x16x64_bf16 v[230:233], v[214:217], v[114:121], v246\n" \
  "v_mov_b64 v[182:183], 0\n" \
  "v_smfmac_f32_16x16x64_bf16 v[234:237], v[214:217], v[122:129], v246\n" \
  "s_waitcnt lgkmcnt(8)\n" \
  "v_mov_b64 v[184:185], 0\n" \
  "v_smfmac_f32_16x16x64_bf16 v[230:233], v[218:221], v[130:137], v246\n" \
  "v_mov_b64 v[186:187], 0\n" \
  "v_smfmac_f32_16x16x64_bf16 v[234:237], v[218:221], v[138:145], v246\n" \
  "buffer_load_dwordx4 v0, s[36:39], s49 offen offset:1024 sc1 lds\n" \
  "v_mov_b64 v[188:189], 0\n" \
  "v_smfmac_f32_16x16x64_bf16 v[230:233], v[222:225], a[0:7], v246\n" \
  "v_mov_b64 v[190:191], 0\n" \
  "v_smfmac_f32_16x16x64_bf16 v[230:233], v[226:229], a[16:23], v246\n" \
  "v_mov_b64 v[192:193], 0\n" \
  "v_smfmac_f32_16x16x64_bf16 v[234:237], v[222:225], a[8:15], v246\n" \
  "buffer_load_dwordx4 v0, s[36:39], s49 offen offset:2048 sc1 lds\n" \
  "v_add_u32 v255, s85, v0\n" \
  "v_smfmac_f32_16x16x64_bf16 v[234:237], v[226:229], a[24:31], v246\n" \
  "s_waitcnt lgkmcnt(4)\n" \
  "v_smfmac_f32_16x16x64_bf16 v[178:181], v[194:197], v[146:153], v246\n" \
  "v_add_f32 v230, v230, v232\n" \
  "v_smfmac_f32_16x16x64_bf16 v[182:185], v[194:197], v[154:161], v246\n" \
  "s_waitcnt lgkmcnt(0)\n" \
  "v_add_f32 v231, v231, v233\n" \
  "v_smfmac_f32_16x16x64_bf16 v[178:181], v[198:201], v[162:169], v246\n" \
  "s_add_u32 s77, s70, s88\n" \
  "v_add_f32 v234, v234, v236\n" \
  "v_smfmac_f32_16x16x64_bf16 v[182:185], v[198:201], v[170:177], v246\n" \
  "buffer_load_dwordx4 v0, s[36:39], s49 offen offset:3072 sc1 lds\n" \
  "v_add_f32 v235, v235, v237\n" \
  "v_smfmac_f32_16x16x64_bf16 v[178:181], v[202:205], a[32:39], v246\n" \
  "ds_read_b128 v[146:149], v255 offset:0\n" \
  "v_cvt_pk_bf16_f32 v232, v230, v231\n" \
  "v_smfmac_f32_16x16x64_bf16 v[182:185], v[202:205], a[40:47], v246\n" \
  "ds_read_b128 v[150:153], v255 offset:1024\n" \
  "v_cvt_pk_bf16_f32 v236, v234, v235\n" \
  "v_smfmac_f32_16x16x64_bf16 v[178:181], v[206:209], a[48:55], v246\n" \
  "ds_write2st64_b32 v247, v232, v236 offset0:36 offset1:37\n" \
  "v_smfmac_f32_16x16x64_bf16 v[182:185], v[206:209], a[56:63], v246\n" \
  "ds_read_b128 v[154:157], v255 offset:2048\n" \
  "v_cmp_u_f32 vcc, v230, v231\n" \
  "v_smfmac_f32_16x16x64_bf16 v[178:181], v[210:213], a[64:71], v246\n" \
  "ds_read_b128 v[158:161], v255 offset:3072\n" \
  "v_mov_b32 v230, s77\n" \
  "v_smfmac_f32_16x16x64_bf16 v[182:185], v[210:213], a[72:79], v246\n" \
  "s_cbranch_vccnz L_CHK%=\n" \
  "L_GO%=:\n" \
  "buffer_store_dword v230, v253, s[36:39], s54 offen sc1\n" \
  "v_smfmac_f32_16x16x64_bf16 v[178:181], v[214:217], a[80:87], v246\n" \
  "v_lshrrev_b32 v177, 6, v247\n" \
  "v_smfmac_f32_16x16x64_bf16 v[182:185], v[214:217], a[88:95], v246\n" \
  "v_lshl_add_u32 v177, v177, 2, v247\n" \
  "s_waitcnt lgkmcnt(2)\n" \
  "s_barrier\n" \
  "ds_read_b64_tr_b16 v[230:231], v248 offset:9216\n" \
  "ds_read_b64_tr_b16 v[232:233], v248 offset:9344\n" \
  "v_smfmac_f32_16x16x64_bf16 v[178:181], v[218:221], a[96:103], v246\n" \
  "ds_read_b64_tr_b16 v[234:235], v248 offset:9728\n" \
  "ds_read_b64_tr_b16 v[236:237], v248 offset:9856\n" \
  "v_smfmac_f32_16x16x64_bf16 v[178:181], v[222:225], a[112:119], v246\n" \
  "ds_read_b64_tr_b16 v[162:163], v248 offset:10240\n" \
  "ds_read_b64_tr_b16 v[164:165], v248 offset:10368\n" \
  "v_smfmac_f32_16x16x64_bf16 v[178:181], v[226:229], a[128:135], v246\n" \
  "ds_read_b64_tr_b16 v[166:167], v248 offset:10752\n" \
  "ds_read_b64_tr_b16 v[168:169], v248 offset:10880\n" \
  "v_smfmac_f32_16x16x64_bf16 v[182:185], v[218:221], a[104:111], v246\n" \
  "v_lshl_add_u32 v177, v177, 1, s58\n" \
  "v_smfmac_f32_16x16x64_bf16 v[182:185], v[222:225], a[120:127], v246\n" \
  "s_nop 0\n" \
  "v_add_f32 v178, v178, v180\n" \
  "v_smfmac_f32_16x16x64_bf16 v[182:185], v[226:229], a[136:143], v246\n" \
  "s_waitcnt lgkmcnt(6)\n" \
  "v_add_f32 v179, v179, v181\n" \
  "v_mfma_f32_16x16x32_bf16 v[170:173], v[146:149], v[230:233], 0\n" \
  "s_waitcnt lgkmcnt(4)\n" \
  "v_cvt_pk_bf16_f32 v178, v178, v179\n" \
  "v_mfma_f32_16x16x32_bf16 v[170:173], v[150:153], v[234:237], v[170:173]\n" \
  "s_waitcnt lgkmcnt(0)\n" \
  "ds_write2_b32 v177, v178, v178 offset0:0 offset1:1\n" \
  "v_mfma_f32_16x16x32_bf16 v[170:173], v[154:157], v[162:165], v[170:173]\n" \
  "v_add_f32 v182, v182, v184\n" \
  "v_add_f32 v183, v183, v185\n" \
  "v_mfma_f32_16x16x32_bf16 v[170:173], v[158:161], v[166:169], v[170:173]\n" \
  "v_cvt_pk_bf16_f32 v182, v182, v183\n" \
  "v_smfmac_f32_16x16x64_bf16 v[186:189], v[194:197], a[144:151], v246\n" \
  "ds_write2_b32 v177, v182, v182 offset0:136 offset1:137\n" \
  "v_smfmac_f32_16x16x64_bf16 v[190:193], v[194:197], a[152:159], v246\n" \
  "ds_read_b128 v[146:149], v255 offset:4096\n" \
  "ds_read_b128 v[150:153], v255 offset:5120\n" \
  "v_smfmac_f32_16x16x64_bf16 v[186:189], v[198:201], a[160:167], v246\n" \
  "ds_read_b128 v[154:157], v255 offset:6144\n" \
  "ds_read_b128 v[158:161], v255 offset:7168\n" \
  "v_smfmac_f32_16x16x64_bf16 v[190:193], v[198:201], a[168:175], v246\n" \
  "v_smfmac_f32_16x16x64_bf16 v[186:189], v[202:205], a[176:183], v246\n" \
  "v_smfmac_f32_16x16x64_bf16 v[190:193], v[202:205], a[184:191], v246\n" \
  "s_waitcnt lgkmcnt(4)\n" \
  "s_barrier\n" \
  "ds_read_b64_tr_b16 v[178:179], v254 offset:11776\n" \
  "ds_read_b64_tr_b16 v[180:181], v254 offset:12048\n" \
  "s_waitcnt lgkmcnt(5)\n" \
  "v_mfma_f32_16x16x32_bf16 v[174:177], v[146:149], v[230:233], 0\n" \
  "ds_read_b64_tr_b16 v[182:183], v254 offset:12864\n" \
  "ds_read_b64_tr_b16 v[184:185], v254 offset:13136\n" \
  "s_waitcnt lgkmcnt(6)\n" \
  "v_mfma_f32_16x16x32_bf16 v[174:177], v[150:153], v[234:237], v[174:177]\n" \
  "ds_read_b64_tr_b16 v[194:195], v254 offset:13952\n" \
  "ds_read_b64_tr_b16 v[196:197], v254 offset:14224\n" \
  "s_waitcnt lgkmcnt(7)\n" \
  "v_mfma_f32_16x16x32_bf16 v[174:177], v[154:157], v[162:165], v[174:177]\n" \
  "ds_read_b64_tr_b16 v[198:199], v254 offset:15040\n" \
  "ds_read_b64_tr_b16 v[200:201], v254 offset:15312\n" \
  "s_waitcnt lgkmcnt(8)\n" \
  "v_mfma_f32_16x16x32_bf16 v[174:177], v[158:161], v[166:169], v[174:177]\n" \
  "v_smfmac_f32_16x16x64_bf16 v[186:189], v[206:209], a[192:199], v246\n" \
  "ds_read_b128 v[146:149], v239 offset:16384\n" \
  "ds_read_b128 v[150:153], v239 offset:17408\n" \
  "v_smfmac_f32_16x16x64_bf16 v[190:193], v[206:209], a[200:207], v246\n" \
  "ds_read_b128 v[154:157], v239 offset:18432\n" \
  "ds_read_b128 v[158:161], v239 offset:19456\n" \
  "v_smfmac_f32_16x16x64_bf16 v[186:189], v[210:213], a[208:215], v246\n" \
  "v_add_u32 v209, s85, v1\n" \
  "v_smfmac_f32_16x16x64_bf16 v[190:193], v[210:213], a[216:223], v246\n" \
  "s_waitcnt lgkmcnt(8)\n" \
  "v_max3_f32 v206, v170, v171, v172\n" \
  "v_mfma_f32_16x16x32_bf16 v[202:205], v[178:181], v[230:233], 0\n" \
  "ds_read_b128 v[210:213], v209 offset:8192\n" \
  "v_max3_f32 v206, v206, v173, v174\n" \
  "v_mfma_f32_16x16x32_bf16 v[202:205], v[182:185], v[234:237], v[202:205]\n" \
  "s_waitcnt lgkmcnt(5)\n" \
  "v_max3_f32 v206, v206, v175, v176\n" \
  "v_mfma_f32_16x16x32_bf16 v[202:205], v[194:197], v[162:165], v[202:205]\n" \
  "ds_read_b128 v[230:233], v239 offset:34816\n" \
  "ds_read_b128 v[234:237], v239 offset:35840\n" \
  "v_mfma_f32_16x16x32_bf16 v[202:205], v[198:201], v[166:169], v[202:205]\n" \
  "v_max_f32 v206, v206, v177\n" \
  "v_lshlrev_b32 v255, 2, v0\n" \
  "v_smfmac_f32_16x16x64_bf16 v[186:189], v[214:217], a[224:231], v246\n" \
  "ds_read_b128 v[162:165], v239 offset:32768\n" \
  "ds_read_b128 v[166:169], v239 offset:33792\n" \
  "v_smfmac_f32_16x16x64_bf16 v[190:193], v[214:217], a[232:239], v246\n" \
  "ds_read_b128 v[198:201], v209 offset:9216\n" \
  "buffer_load_dword v255, v255, s[36:39], s79 offen sc1\n" \
  "v_max3_f32 v207, v206, v202, v203\n" \
  "v_max3_f32 v206, v206, v202, v203\n" \
  "v_smfmac_f32_16x16x64_bf16 v[186:189], v[218:221], a[240:247], v246\n" \
  "s_nop 0\n" \
  "v_permlane16_swap_b32 v206, v207\n" \
  "v_max_f32 v208, v206, v207\n" \
  "v_max_f32 v207, v206, v207\n" \
  "v_smfmac_f32_16x16x64_bf16 v[190:193], v[218:221], a[248:255], v246\n" \
  "s_nop 0\n" \
  "v_permlane32_swap_b32 v208, v207\n" \
  "v_max_f32 v206, v208, v207\n" \
  "v_mul_f32 v208, s62, v206\n" \
  "v_pk_fma_f32 v[170:171], v[170:171], s[62:63], v[208:209] op_sel_hi:[1,0,0] neg_lo:[0,0,1] neg_hi:[0,0,1]\n" \
  "v_pk_fma_f32 v[172:173], v[172:173], s[62:63], v[208:209] op_sel_hi:[1,0,0] neg_lo:[0,0,1] neg_hi:[0,0,1]\n" \
  "v_pk_fma_f32 v[174:175], v[174:175], s[62:63], v[208:209] op_sel_hi:[1,0,0] neg_lo:[0,0,1] neg_hi:[0,0,1]\n" \
  "v_pk_fma_f32 v[176:177], v[176:177], s[62:63], v[208:209] op_sel_hi:[1,0,0] neg_lo:[0,0,1] neg_hi:[0,0,1]\n" \
  "v_pk_fma_f32 v[202:203], v[202:203], s[62:63], v[208:209] op_sel_hi:[1,0,0] neg_lo:[0,0,1] neg_hi:[0,0,1]\n" \
  "s_waitcnt lgkmcnt(8)\n" \
  "v_smfmac_f32_16x16x64_bf16 v[186:189], v[222:225], v[146:153], v246\n" \
  "v_exp_f32 v170, v170\n" \
  "v_exp_f32 v171, v171\n" \
  "s_waitcnt lgkmcnt(6)\n" \
  "v_smfmac_f32_16x16x64_bf16 v[190:193], v[222:225], v[154:161], v246\n" \
  "v_exp_f32 v172, v172\n" \
  "v_exp_f32 v173, v173\n" \
  "v_cvt_pk_bf16_f32 v170, v170, v171\n" \
  "s_waitcnt lgkmcnt(3)\n" \
  "v_smfmac_f32_16x16x64_bf16 v[190:193], v[226:229], v[230:237], v246\n" \
  "v_exp_f32 v174, v174\n" \
  "v_exp_f32 v175, v175\n" \
  "v_cvt_pk_bf16_f32 v171, v172, v173\n" \
  "s_waitcnt lgkmcnt(1)\n" \
  "v_smfmac_f32_16x16x64_bf16 v[186:189], v[226:229], v[162:169], v246\n" \
  "v_exp_f32 v176, v176\n" \
  "v_exp_f32 v177, v177\n" \
  "v_cvt_pk_bf16_f32 v172, v174, v175\n" \
  "v_exp_f32 v202, v202\n" \
  "v_cvt_pk_bf16_f32 v173, v176, v177\n" \
  "v_exp_f32 v203, v203\n" \
  "s_nop 0\n" \
  "v_mfma_f32_16x16x32_bf16 v[194:197], v[240:243], v[170:173], 0\n" \
  "v_cvt_pk_bf16_f32 v204, v202, v203\n" \
  "v_cvt_pk_bf16_f32 v205, v202, v203\n" \
  "s_waitcnt lgkmcnt(0)\n" \
  "v_mfma_f32_16x16x32_bf16 v[218:221], v[198:201], v[170:173], 0\n" \
  "v_cvt_pk_bf16_f32 v176, v190, v191\n" \
  "v_cvt_pk_bf16_f32 v177, v192, v193\n" \
  "v_mfma_f32_16x16x32_bf16 v[214:217], v[210:213], v[170:173], 0\n" \
  "v_cvt_pk_bf16_f32 v174, v186, v187\n" \
  "v_cvt_pk_bf16_f32 v175, v188, v189\n" \
  "v_mfma_f32_16x16x16_bf16 v[194:197], v[244:245], v[204:205], v[194:197]\n" \
  "v_mov_b64 v[146:147], 0\n" \
  "v_mov_b64 v[148:149], 0\n" \
  "v_mov_b64 v[154:155], 0\n" \
  "v_mfma_f32_16x16x16_bf16 v[218:221], v[176:177], v[204:205], v[218:221]\n" \
  "v_mov_b64 v[156:157], 0\n" \
  "v_mov_b64 v[150:151], 0\n" \
  "v_mov_b64 v[152:153], 0\n" \
  "v_mfma_f32_16x16x16_bf16 v[214:217], v[174:175], v[204:205], v[214:217]\n" \
  "s_mov_b64 vcc, s[74:75]\n" \
  "v_rcp_f32 v208, v194\n" \
  "v_mov_b64 v[158:159], 0\n" \
  "v_mov_b64 v[160:161], 0\n" \
  "v_mov_b64 v[178:179], 0\n" \
  "v_mov_b64 v[180:181], 0\n" \
  "s_cmp_eq_u32 s65, 63\n" \
  "s_nop 0\n" \
  "v_cndmask_b32 v214, v214, v218, vcc\n" \
  "v_cndmask_b32 v215, v215, v219, vcc\n" \
  "v_cndmask_b32 v216, v216, v220, vcc\n" \
  "v_cndmask_b32 v217, v217, v221, vcc\n" \
  "v_pk_mul_f32 v[214:215], v[214:215], v[208:209] op_sel_hi:[1,0]\n" \
  "v_pk_mul_f32 v[216:217], v[216:217], v[208:209] op_sel_hi:[1,0]\n" \
  "v_cvt_pk_bf16_f32 v214, v214, v215\n" \
  "v_cvt_pk_bf16_f32 v215, v216, v217\n" \
  "ds_write_b64 v249, v[214:215] offset:9216\n" \
  "s_waitcnt lgkmcnt(0)\n" \
  "s_barrier\n" \
  "ds_read_b128 v[222:225], v250 offset:9216\n" \
  "ds_read_b128 v[226:229], v250 offset:9344\n" \
  "s_cbranch_scc1 L_EXP%=\n" \
  "s_add_u32 s69, s70, s56\n" \
  "s_waitcnt lgkmcnt(1)\n" \
  "v_smfmac_f32_16x16x64_bf16 v[146:149], v[222:225], v[2:9], v246\n" \
  "s_waitcnt lgkmcnt(0)\n" \
  "v_smfmac_f32_16x16x64_bf16 v[146:149], v[226:229], v[10:17], v246\n" \
  "v_smfmac_f32_16x16x64_bf16 v[154:157], v[222:225], v[34:41], v246\n" \
  "v_smfmac_f32_16x16x64_bf16 v[154:157], v[226:229], v[42:49], v246\n" \
  "s_nop 0\n" \
  "v_add_f32 v146, v146, v148\n" \
  "v_smfmac_f32_16x16x64_bf16 v[150:153], v[222:225], v[18:25], v246\n" \
  "v_add_f32 v147, v147, v149\n" \
  "v_smfmac_f32_16x16x64_bf16 v[150:153], v[226:229], v[26:33], v246\n" \
  "v_cvt_pk_bf16_f32 v146, v146, v147\n" \
  "v_smfmac_f32_16x16x64_bf16 v[158:161], v[222:225], v[50:57], v246\n" \
  "v_add_f32 v154, v154, v156\n" \
  "v_smfmac_f32_16x16x64_bf16 v[158:161], v[226:229], v[58:65], v246\n" \
  "v_add_f32 v155, v155, v157\n" \
  "v_smfmac_f32_16x16x64_bf16 v[178:181], v[222:225], v[66:73], v246\n" \
  "v_cvt_pk_bf16_f32 v148, v154, v155\n" \
  "v_smfmac_f32_16x16x64_bf16 v[178:181], v[226:229], v[74:81], v246\n" \
  "v_add_f32 v150, v150, v152\n" \
  "v_add_f32 v151, v151, v153\n" \
  "v_permlane16_swap_b32 v146, v148\n" \
  "v_cvt_pk_bf16_f32 v147, v150, v151\n" \
  "v_pk_add_f32 v[158:159], v[158:159], v[160:161]\n" \
  "v_cvt_pk_bf16_f32 v149, v158, v159\n" \
  "s_nop 1\n" \
  "v_pk_add_f32 v[178:179], v[178:179], v[180:181]\n" \
  "v_cvt_pk_bf16_f32 v178, v178, v179\n" \
  "v_permlane16_swap_b32 v147, v149\n" \
  "buffer_store_dwordx4 v[146:149], v251, s[36:39], s78 offen sc1\n" \
  "buffer_store_dword v178, v252, s[36:39], s78 offen sc1\n" \
  "s_memrealtime s[72:73]\n" \
  "s_waitcnt vmcnt(2)\n" \
  "v_subrev_u32 v255, s70, v255\n" \
  "s_nop 1\n" \
  "v_max_i32_dpp v255, v255, v255 row_ror:1 row_mask:0xf bank_mask:0xf\n" \
  "s_nop 1\n" \
  "v_max_i32_dpp v255, v255, v255 row_ror:2 row_mask:0xf bank_mask:0xf\n" \
  "s_nop 1\n" \
  "v_max_i32_dpp v255, v255, v255 row_ror:4 row_mask:0xf bank_mask:0xf\n" \
  "s_nop 1\n" \
  "v_max_i32_dpp v255, v255, v255 row_ror:8 row_mask:0xf bank_mask:0xf\n" \
  "s_nop 0\n" \
  "v_readlane_b32 s76, v255, 0\n" \
  "v_readlane_b32 s77, v255, 16\n" \
  "v_readlane_b32 s84, v255, 32\n" \
  "v_readlane_b32 s68, v255, 48\n" \
  "s_add_u32 s79, s47, s67\n" \
  "s_max_i32 s76, s76, s77\n" \
  "s_max_i32 s84, s84, s68\n" \
  "s_max_i32 s76, s76, s88\n" \
  "s_max_i32 s84, s84, s76\n" \
  "s_min_i32 s84, s84, 640\n" \
  "s_add_u32 s69, s69, s84\n" \
  "s_waitcnt lgkmcnt(0)\n" \
  "s_sub_u32 s76, s72, s70\n" \
  "s_min_u32 s76, s76, 256\n" \
  "s_lshr_b32 s77, s89, 1\n" \
  "s_add_u32 s89, s77, s76\n" \
  "s_lshr_b32 s88, s89, 1\n" \
  "s_sub_u32 s76, s88, s86\n" \
  "s_max_i32 s76, s76, 0\n" \
  "s_mul_i32 s76, s76, s87\n" \
  "s_lshr_b32 s76, s76, 2\n" \
  "s_add_u32 s68, s69, s57\n" \
  "s_add_u32 s68, s68, s76\n" \
  "s_bitcmp1_b32 s59, 0\n" \
  "s_cbranch_scc0 L_EXP%=\n" \
  "s_bitset1_b32 s59, 3\n" \
  "s_mov_b32 s76, 0\n" \
  "L_U1%=:\n" \
  "s_memrealtime s[72:73]\n" \
  "s_add_u32 s76, s76, 0x400000\n" \
  "s_cbranch_scc1 L_U1%=x\n" \
  "s_waitcnt lgkmcnt(0)\n" \
  "s_sub_u32 s77, s72, s69\n" \
  "s_cmp_lt_i32 s77, 0\n" \
  "s_cbranch_scc1 L_U1%=\n" \
  "L_U1%=x:\n" \
  "buffer_load_dwordx4 v[210:213], v0, s[36:39], s79 offen offset:0 sc1\n" \
  "buffer_load_dwordx4 v[214:217], v0, s[36:39], s79 offen offset:1024 sc1\n" \
  "buffer_load_dwordx4 v[218:221], v0, s[36:39], s79 offen offset:2048 sc1\n" \
  "buffer_load_dwordx4 v[222:225], v0, s[36:39], s79 offen offset:3072 sc1\n" \
  "L_EXP%=:\n" \
  "s_bitcmp1_b32 s59, 1\n" \
  "s_cbranch_scc0 L_EXK%=\n" \
  "buffer_store_dwordx4 v[174:177], v0, s[36:39], s51 offen sc1\n" \
  "s_branch L_EXD%=\n" \
  "L_EXK%=:\n" \
  "v_lshrrev_b32 v194, 11, v1\n" \
  "v_mul_u32_u24 v194, 0x440, v194\n" \
  "v_add_u32 v194, v194, v254\n" \
  "ds_read_b64_tr_b16 v[198:199], v194 offset:11776\n" \
  "ds_read_b64_tr_b16 v[200:201], v194 offset:12048\n" \
  "v_lshrrev_b32 v195, 1, v0\n" \
  "v_and_b32 v195, 0x1e0, v195\n" \
  "v_and_or_b32 v195, v0, 16, v195\n" \
  "v_and_b32 v196, 32, v0\n" \
  "v_lshl_or_b32 v195, v196, 26, v195\n" \
  "s_waitcnt lgkmcnt(0)\n" \
  "buffer_store_dwordx4 v[198:201], v195, s[36:39], s51 offen sc1\n" \
  "L_EXD%=:\n" \
  "s_add_u32 s51, s51, s52\n" \
  "s_and_b32 s85, s50, 0xc000\n" \
  "s_xor_b32 s50, s50, 0xc000\n" \
  "s_add_u32 s49, s49, 0x40000\n" \
  "s_bitcmp1_b32 s59, 2\n" \
  "s_cbranch_scc0 L_NOF%=\n" \
  "s_mov_b64 exec, 1\n" \
  "v_mov_b32 v194, 1\n" \
  "buffer_store_dword v194, off, s[36:39], s53 sc1\n" \
  "s_mov_b64 exec, -1\n" \
  "L_NOF%=:\n" \
  "s_add_u32 s53, s53, 4096\n" \
  "s_add_u32 s65, s65, 1\n" \
  "s_cmp_lt_u32 s65, 64\n" \
  "s_cbranch_scc1 L_PRE%=\n" \
  "s_waitcnt vmcnt(0) lgkmcnt(0)\n" \
  "s_barrier\n" \
  "s_bitcmp1_b32 s59, 2\n" \
  "s_cbranch_scc0 L_FIN%=\n" \
  "s_mov_b64 exec, 1\n" \
  "v_mov_b32 v194, 1\n" \
  "buffer_store_dword v194, off, s[36:39], s53 sc1\n" \
  "s_mov_b64 exec, -1\n" \
  "L_FIN%=:\n" \
  "s_branch L_END%=\n" \
  "L_KA%=:\n" \
  "v_and_b32 v194, 0x3c0, v0\n" \
  "v_and_b32 v195, 16, v0\n" \
  "v_lshlrev_b32 v194, 1, v194\n" \
  "v_lshl_or_b32 v194, v195, 2, v194\n" \
  "v_bfe_u32 v195, v0, 5, 1\n" \
  "v_lshl_or_b32 v194, v195, 2, v194\n" \
  "s_mov_b32 s78, 0\n" \
  "L_KA%=W:\n" \
  "buffer_load_dword v195, v194, s[36:39], s55 offen sc1\n" \
  "s_waitcnt vmcnt(0)\n" \
  "v_cmp_eq_u32 vcc, -1, v195\n" \
  "s_nop 4\n" \
  "s_ff1_i32_b64 s66, vcc\n" \
  "s_min_u32 s66, s66, 64\n" \
  "s_and_b32 s66, s66, 0x7e\n" \
  "s_cmp_gt_u32 s66, s77\n" \
  "s_cbranch_scc1 L_KAB%=\n" \
  "s_sleep 2\n" \
  "s_add_u32 s78, s78, 1\n" \
  "s_cmp_lt_u32 s78, %[smax]\n" \
  "s_cbranch_scc1 L_KA%=W\n" \
  "s_branch L_KAB%=\n" \
  "L_KV%=:\n" \
  "v_and_b32 v194, 0x3c0, v0\n" \
  "v_and_b32 v195, 16, v0\n" \
  "v_lshlrev_b32 v194, 1, v194\n" \
  "v_lshl_or_b32 v194, v195, 2, v194\n" \
  "v_bfe_u32 v195, v0, 5, 1\n" \
  "v_lshl_or_b32 v194, v195, 2, v194\n" \
  "s_mov_b32 s78, 0\n" \
  "L_KV%=W:\n" \
  "buffer_load_dword v195, v194, s[36:39], s55 offen sc1\n" \
  "s_waitcnt vmcnt(0)\n" \
  "v_cmp_eq_u32 vcc, -1, v195\n" \
  "s_nop 4\n" \
  "s_ff1_i32_b64 s66, vcc\n" \
  "s_min_u32 s66, s66, 64\n" \
  "s_and_b32 s66, s66, 0x7e\n" \
  "s_cmp_gt_u32 s66, s77\n" \
  "s_cbranch_scc1 L_KVB%=\n" \
  "s_sleep 2\n" \
  "s_add_u32 s78, s78, 1\n" \
  "s_cmp_lt_u32 s78, %[smax]\n" \
  "s_cbranch_scc1 L_KV%=W\n" \
  "s_branch L_KVB%=\n" \
  "L_RSR%=:\n" \
  "s_add_u32 s83, s83, 1\n" \
  "s_cmp_lt_u32 s83, %[smax]\n" \
  "s_cbranch_scc0 L_RSOK%=\n" \
  "buffer_load_dwordx4 v[210:213], v0, s[36:39], s79 offen offset:0 sc1\n" \
  "buffer_load_dwordx4 v[214:217], v0, s[36:39], s79 offen offset:1024 sc1\n" \
  "buffer_load_dwordx4 v[218:221], v0, s[36:39], s79 offen offset:2048 sc1\n" \
  "buffer_load_dwordx4 v[222:225], v0, s[36:39], s79 offen offset:3072 sc1\n" \
  "s_waitcnt vmcnt(3)\n" \
  "v_mfma_f32_16x16x32_bf16 v[206:209], v[210:213], v[226:229], 0\n" \
  "v_max3_u32 v194, v210, v211, v212\n" \
  "v_max_u32 v194, v194, v213\n" \
  "s_waitcnt vmcnt(2)\n" \
  "v_mfma_f32_16x16x32_bf16 v[206:209], v[214:217], v[226:229], v[206:209]\n" \
  "v_max3_u32 v194, v194, v214, v215\n" \
  "v_max3_u32 v194, v194, v216, v217\n" \
  "s_waitcnt vmcnt(1)\n" \
  "v_mfma_f32_16x16x32_bf16 v[206:209], v[218:221], v[226:229], v[206:209]\n" \
  "v_max3_u32 v194, v194, v218, v219\n" \
  "v_max3_u32 v194, v194, v220, v221\n" \
  "s_waitcnt vmcnt(0)\n" \
  "v_mfma_f32_16x16x32_bf16 v[206:209], v[222:225], v[226:229], v[206:209]\n" \
  "v_max3_u32 v194, v194, v222, v223\n" \
  "v_max3_u32 v194, v194, v224, v225\n" \
  "v_cmp_eq_u32 vcc, -1, v194\n" \
  "s_cbranch_vccnz L_RSR%=\n" \
  "s_branch L_RSOK%=\n" \
  "L_CHK%=:\n" \
  "v_max3_u32 v234, v194, v195, v196\n" \
  "v_max_u32 v234, v234, v197\n" \
  "v_max3_u32 v234, v234, v198, v199\n" \
  "v_max3_u32 v234, v234, v200, v201\n" \
  "v_max3_u32 v234, v234, v202, v203\n" \
  "v_max3_u32 v234, v234, v204, v205\n" \
  "v_max3_u32 v234, v234, v206, v207\n" \
  "v_max3_u32 v234, v234, v208, v209\n" \
  "v_max3_u32 v234, v234, v210, v211\n" \
  "v_max3_u32 v234, v234, v212, v213\n" \
  "v_max3_u32 v234, v234, v214, v215\n" \
  "v_max3_u32 v234, v234, v216, v217\n" \
  "v_max3_u32 v234, v234, v218, v219\n" \
  "v_max3_u32 v234, v234, v220, v221\n" \
  "v_max3_u32 v234, v234, v222, v223\n" \
  "v_max3_u32 v234, v234, v224, v225\n" \
  "v_max3_u32 v234, v234, v226, v227\n" \
  "v_max3_u32 v234, v234, v228, v229\n" \
  "v_cmp_eq_u32 vcc, -1, v234\n" \
  "s_cbranch_vccz L_GO%=\n" \
  "s_add_u32 s80, s80, 1\n" \
  "s_cmp_lt_u32 s80, %[smax]\n" \
  "s_cbranch_scc0 L_GO%=\n" \
  "s_memrealtime s[72:73]\n" \
  "s_barrier\n" \
  "ds_read_b128 v[146:149], v238 offset:0\n" \
  "ds_read_b128 v[150:153], v238 offset:1024\n" \
  "ds_read_b128 v[154:157], v238 offset:2048\n" \
  "ds_read_b128 v[158:161], v238 offset:3072\n" \
  "ds_read_b128 v[162:165], v238 offset:16384\n" \
  "ds_read_b128 v[166:169], v238 offset:17408\n" \
  "ds_read_b128 v[170:173], v238 offset:18432\n" \
  "ds_read_b128 v[174:177], v238 offset:19456\n" \
  "ds_read_b128 v[178:181], v238 offset:32768\n" \
  "ds_read_b128 v[182:185], v238 offset:33792\n" \
  "ds_read_b128 v[186:189], v238 offset:34816\n" \
  "ds_read_b128 v[190:193], v238 offset:35840\n" \
  "v_mov_b64 v[230:231], 0\n" \
  "v_mov_b64 v[232:233], 0\n" \
  "v_mov_b64 v[234:235], 0\n" \
  "v_mov_b64 v[236:237], 0\n" \
  "s_bitcmp1_b32 s59, 4\n" \
  "s_cbranch_scc0 L_FB%=\n" \
  "s_cmp_eq_u32 s80, 1\n" \
  "s_cselect_b32 s76, 0x10000, 0\n" \
  "s_add_u32 s81, s81, s76\n" \
  "s_lshr_b32 s76, s76, 4\n" \
  "s_add_u32 s82, s82, s76\n" \
  "s_mov_b32 m0, s60\n" \
  "s_nop 0\n" \
  "buffer_load_dwordx4 v0, s[36:39], s81 offen offset:0 sc1 lds\n" \
  "buffer_load_dwordx4 v0, s[36:39], s81 offen offset:1024 sc1 lds\n" \
  "buffer_load_dwordx4 v0, s[36:39], s81 offen offset:2048 sc1 lds\n" \
  "buffer_load_dwordx4 v0, s[36:39], s81 offen offset:3072 sc1 lds\n" \
  "s_mov_b32 m0, s61\n" \
  "s_mov_b32 exec_hi, 0\n" \
  "buffer_load_dwordx4 v0, s[36:39], s82 offen sc1 lds\n" \
  "s_mov_b32 exec_hi, -1\n" \
  "s_waitcnt lgkmcnt(0)\n" \
  "s_add_u32 s76, s72, s88\n" \
  "s_add_u32 s76, s76, 36\n" \
  "v_mov_b32 v194, s76\n" \
  "buffer_store_dword v194, v253, s[36:39], s54 offen sc1\n" \
  "s_waitcnt vmcnt(1)\n" \
  "L_FB%=:\n" \
  "s_waitcnt lgkmcnt(0)\n" \
  "s_barrier\n" \
  "s_branch L_STEP%=\n" \
  "L_END%=:\n" \
  ""
#define R_LOOP_CLOBBERS "v146", "v147", "v148", "v149", "v150", "v151", "v152", "v153", "v154", "v155", "v156", "v157", "v158", "v159", "v160", "v161", "v162", "v163", "v164", "v165", "v166", "v167", "v168", "v169", "v170", "v171", "v172", "v173", "v174", "v175", "v176", "v177", "v178", "v179", "v180", "v181", "v182", "v183", "v184", "v185", "v186", "v187", "v188", "v189", "v190", "v191", "v192", "v193", "v194", "v195", "v196", "v197", "v198", "v199", "v200", "v201", "v202", "v203", "v204", "v205", "v206", "v207", "v208", "v209", "v210", "v211", "v212", "v213", "v214", "v215", "v216", "v217", "v218", "v219", "v220", "v221", "v222", "v223", "v224", "v225", "v226", "v227", "v228", "v229", "v230", "v231", "v232", "v233", "v234", "v235", "v236", "v237", "s68", "s69", "s70", "s71", "s72", "s73", "s74", "s75", "s76", "s77", "s78", "s79", "s80", "s81", "s82", "s83", "s84", "s85", "s86", "s87", "s88", "s89", "s90", "vcc", "scc", "memory"

struct Prm {
  const u16 *x, *mem0, *Wq, *Wk, *Wv, *Wo, *wg, *wu, *wd;
  unsigned char* ws;
  u16* out;
  int d1, d2, d3, d4, mode, pad;
};
constexpr unsigned L_AGB = 0, L_QB = L_AGB + 9216, L_OB = L_QB, L_KB = L_QB + 2560, L_KV = 16384, L_WS = 49152;
static_assert(L_KB + 4352 <= L_KV && L_AGB == 0, "lds");
constexpr unsigned slot_addr(int j) { return ((j + 3) % 10) * 16384; }
typedef unsigned long long u64;
typedef u64 ul2 __attribute__((ext_vector_type(2)));
DI f4 zero4() { ul2 z; asm volatile("v_mov_b64 %0, 0" : "=v"(z[0])); asm volatile("v_mov_b64 %0, 0" : "=v"(z[1])); return __builtin_bit_cast(f4, z); }
DI unsigned opq(unsigned v) { asm volatile("" : "+v"(v)); return v; }
DI unsigned max3u(unsigned a, unsigned b, unsigned c) { return max(max(a, b), c); }
DI f4 amf32z(bf8 a, bf8 b) { f4 c; asm volatile("v_mfma_f32_16x16x32_bf16 %0, %1, %2, 0" : "=&v"(c) : "v"(a), "v"(b)); return c; }
DI void amf32(f4& c, bf8 a, bf8 b) { asm volatile("v_mfma_f32_16x16x32_bf16 %0, %1, %2, %0" : "+v"(c) : "v"(a), "v"(b)); }
DI void amf16(f4& c, u2 a, u2 b) { asm volatile("v_mfma_f32_16x16x16_bf16 %0, %1, %2, %0" : "+v"(c) : "v"(a), "v"(b)); }
DI void smfv(f4& c, bf8 a, bf16x16 b, int idx) { asm volatile("v_smfmac_f32_16x16x64_bf16 %0, %1, %2, %3" : "+v"(c) : "v"(a), "v"(b), "v"(idx)); }
DI void smfa(f4& c, bf8 a, bf16x16 b, int idx) { asm volatile("v_smfmac_f32_16x16x64_bf16 %0, %1, %2, %3" : "+v"(c) : "v"(a), "a"(b), "v"(idx)); }
template <int N> DI void nop() { asm volatile("s_nop %0" ::"n"(N)); }
template <int N> DI void pin(f4& a) { asm volatile("s_nop %1" : "+v"(a) : "n"(N)); }
template <int N> DI void pin(f4& a, f4& b) { asm volatile("s_nop %2" : "+v"(a), "+v"(b) : "n"(N)); }

template <int RS> DI bf16x16 wfrag(LAS char* L, unsigned a, unsigned off) { return cat16(trd(L + off, a), trd(L + off + 2 * RS, a), trd(L + off + 32 * RS, a), trd(L + off + 34 * RS, a)); }
DI bf8 sfrag(LAS char* L, unsigned a, unsigned off) { return cat8(trd(L + off, a), trd(L + off + 128, a)); }
DI bf8 rd16(LAS char* L, unsigned a, unsigned off) { return *(LAS bf8*)(L + off + a); }
DI bf16x16 lfrag(LAS char* L, unsigned a, unsigned off) { return __builtin_shufflevector(rd16(L, a, off), rd16(L, a, off + 1024), 0, 1, 2, 3, 4, 5, 6, 7, 8, 9, 10, 11, 12, 13, 14, 15); }

struct LdE { int m, ks, slot; };
constexpr LdE res_chunk(int j) { return j < 6 ? LdE{0, 3 + j, 0} : (j < 13 ? LdE{1, 2 + (j - 6), 0} : LdE{2, j - 13, 0}); }
constexpr LdE str_chunk(int j) { return j < 3 ? LdE{0, j, j} : (j < 5 ? LdE{1, j - 3, j} : LdE{2, 7 + (j - 5), j}); }
constexpr int res_slot(int j) { return j < 5 ? 5 + j : (j < 10 ? j - 5 : (j < 15 ? j - 5 : j - 10)); }
constexpr LdE ld_list(int i) {
  LdE e = {0, 0, 0};
  if (i < 15) { e = res_chunk(i); e.slot = res_slot(i); }
  else if (i < 20) e = str_chunk(i - 15);
  else if (i < 25) { e = res_chunk(i - 5); e.slot = res_slot(i - 5); }
  else e = str_chunk(i - 20);
  return e;
}

DI unsigned rtime() { u64 t; asm volatile("s_memrealtime %0\n\ts_waitcnt lgkmcnt(0)" : "=s"(t) :: "memory"); return (unsigned)t; }
DI unsigned until(unsigned t) { unsigned now; int it = 0; do now = rtime(); while ((int)(now - t) < 0 && ++it < 1024); return now; }
DI unsigned tmax(unsigned a, unsigned b) { return (int)(a - b) > 0 ? a : b; }
typedef unsigned u8x __attribute__((ext_vector_type(8)));
typedef unsigned u16x __attribute__((ext_vector_type(16)));
typedef unsigned u32x __attribute__((ext_vector_type(32)));
DI u16x c2(const bf16x16 (&a)[2]) { return __builtin_shufflevector(__builtin_bit_cast(u8x, a[0]), __builtin_bit_cast(u8x, a[1]), 0, 1, 2, 3, 4, 5, 6, 7, 8, 9, 10, 11, 12, 13, 14, 15); }
DI u32x c4(const bf16x16 (&a)[2], const bf16x16 (&b)[2]) {
  return __builtin_shufflevector(c2(a), c2(b), 0, 1, 2, 3, 4, 5, 6, 7, 8, 9, 10, 11, 12, 13, 14, 15, 16, 17, 18, 19, 20, 21, 22, 23, 24, 25, 26, 27, 28, 29, 30, 31);
}
DI void role_r(const Prm& p, LAS char* L, const int rid) {
  const int w = __builtin_amdgcn_readfirstlane(threadIdx.x >> 6), l = threadIdx.x & 63;
  const int n = l & 15, g = l >> 4, qq = (l >> 2) & 3, pp = l & 3;
  const int head = rid >> 1, hf = rid & 1, G = 4 * hf + w;
  const rsrc_t rws = mk_rsrc(p.ws, WS_BYTES);
  const bool two = (rid & 7) == 7, isred = (w == 0) || (w == 1 && two);
  const int task = w == 0 ? rid : 64 + (rid >> 3);
  const unsigned l16 = l * 16;
  unsigned sel = 0;
  bf16x16 Wx[9][2], Wy[9][2], Wv[9][2], Wo[5][2];
  const unsigned wb = opq(L_WS + w * 4096 + l16);
  WST(rid, 101);
  const unsigned vTB = (w == 3 && l < 2) ? l * 4096 + rid * 64 : 0x80000000u;
  {
    const unsigned db = 16 * (g >> 1) + 4 * (g & 1) + 8 * (qq >> 1) + (qq & 1);
    const unsigned fsw = (qq & 1) | ((g & 1) << 1) | ((qq >> 1) << 2);
    const unsigned wa0 = db * 256 + (((2 * w) ^ fsw) * 32) + pp * 8, wa1 = db * 256 + (((2 * w + 1) ^ fsw) * 32) + pp * 8;
    const rsrc_t rq = mk_rsrc(p.Wq, D_ * HE_ * 2), rk = mk_rsrc(p.Wk, D_ * HE_ * 2), rv = mk_rsrc(p.Wv, D_ * HE_ * 2), ro = mk_rsrc(p.Wo, D_ * HE_ * 2);
#pragma unroll
    for (int i = 0; i < 20; ++i) {
      const unsigned pi = 64 * i + l, row = pi / 10, pc = pi - row * 10;
      dma_i(w * 20480, L_WS + i * 1024, (head * 128 + row) * 1152 + (pc < 8 ? 64 * G + 32 * (pc >> 2) + 16 * (pc & 1) + 4 * (pc & 2) : 448 + 16 * w + 8 * pc) * 2, ro, 0);
    }
    const unsigned vo = (16 * w + g) * 8192 + head * 256 + (((n >> 1) ^ (g & 1)) * 32) + (n & 1) * 16;
    auto issue = [&](int i) {
      const LdE e = ld_list(i);
#pragma unroll
      for (int k = 0; k < 4; ++k) dma_i(w * 4096, slot_addr(e.slot) + k * 1024, vo ^ (k * 64), e.m == 0 ? rq : (e.m == 1 ? rk : rv), (e.ks * 64 + 4 * k) * 8192);
    };
#pragma unroll
    for (int i = 0; i < 5; ++i) issue(i);
    wvm<20>();
    const unsigned oa = opq(L_WS + w * 20480 + db * 160 + pp * 8);
#pragma unroll
    for (int t = 0; t < 5; ++t)
#pragma unroll
      for (int k2 = 0; k2 < 2; ++k2) Wo[t][k2] = wfrag<160>(L, oa, t * 32 + k2 * 64 * 160);
#pragma unroll
    for (int j = 0; j < 20; ++j) {
      if (j == 0) wvm<16>(); else if (j < 15) wvm<32>(); else if (j < 19) wvm<12>(); else wvm<8>();
      bar();
      if (j == 0) {
#pragma unroll
        for (int i = 5; i < 10; ++i) issue(i);
      } else if (j + 9 < 27) issue(j + 9);
      const LdE e = res_chunk(j);
      const unsigned sa = slot_addr(res_slot(j));
      bf16x16 f0 = wfrag<256>(L, opq(wa0 + sa), 0), f1 = wfrag<256>(L, opq(wa1 + sa), 0);
      if (e.m != 0 || e.ks >= 7) asm volatile("" : "+a"(f0), "+a"(f1));
      if (e.m == 0) { Wx[e.ks][0] = f0; Wx[e.ks][1] = f1; }
      else if (e.m == 1) { Wy[e.ks][0] = f0; Wy[e.ks][1] = f1; }
      else { Wv[e.ks][0] = f0; Wv[e.ks][1] = f1; }
    }
    wvm<0>();
    bar();
    const unsigned vfc = w < 2 ? (128 + l) * 64 + w * 16 : (l & 31) * 64;
    const u2 gen = ld8(rws, 0, OFF_GEN), sl = ld8(rws, 0, OFF_SEL);
    const u4 fc = ld16(rws, vfc, OFF_FX);
    asm volatile("" ::: "memory");
#pragma unroll
    for (int b = 0; b < 2; ++b) {
      bf16x16 f[4][2];
#pragma unroll
      for (int j = 0; j < 4 - b; ++j) { const unsigned sa = slot_addr(4 * b + j); f[j][0] = wfrag<256>(L, opq(wa0 + sa), 0); f[j][1] = wfrag<256>(L, opq(wa1 + sa), 0); }
      bar();
      const unsigned wd = opq(wb + b * 65536);
#pragma unroll
      for (int j = 0; j < 4 - b; ++j)
#pragma unroll
        for (int t = 0; t < 2; ++t) {
          *(LAS bf8*)(L + wd + j * 16384 + t * 2048) = __builtin_shufflevector(f[j][t], f[j][t], 0, 1, 2, 3, 4, 5, 6, 7);
          *(LAS bf8*)(L + wd + j * 16384 + t * 2048 + 1024) = __builtin_shufflevector(f[j][t], f[j][t], 8, 9, 10, 11, 12, 13, 14, 15);
        }
    }
    asm volatile("" ::: "memory");
    const u2 tag = tag_of(gen);
    if (!tagged(fc, tag)) wait_fx(rws, vfc, tag);
    u2 sv = sl;
    for (int sp = 0; sp < SPIN_MAX && __ballot(sv.x != tag.x || ((sv.y ^ tag.y) & ~8192u)); ++sp) sv = ld8(rws, 0, OFF_SEL);
    sel = (__builtin_amdgcn_readfirstlane(sv.y) ^ tag.y) & 8192u;
    bar_nowait();
  }
  const unsigned agt = OFF_AG + sel + (task < 64 ? task * 128 : 8 * 16384 + (task - 64) * 128);
  const unsigned rst = OFF_RS + sel + (task >> 1) * 16384 + (task & 1) * 4096;
  const unsigned idx = (l & 2) ? 0xEEEE : 0x4444;
  const unsigned aQW = 512 * w + 16 * n + 4 * g;
  const unsigned aQR = (16 * (g >> 1) + 4 * (g & 1) + qq) * 16 + 8 * (l & 1);
  const unsigned aKR = (4 * (g >> 1) + (g & 1)) * 136 + qq * 32 + 8 * pp;
  const unsigned aOW = (n & 7) * 288 + (32 * w + 16 * (n >> 3) + 4 * g) * 2;
  const unsigned aOR = (2 * (n >> 2) + (n & 1)) * 288 + (16 * g + 8 * ((n >> 1) & 1)) * 2;
  const unsigned vRS4 = (2 * (g & 1) + (n >> 3)) * 16384 + (g >> 1) * 4096 + (n & 7) * 16;
  const unsigned vRS1 = hf ? 0x80000000u : (32 - 3 * w) * 16384 + (g >> 1) * 4096 + (n & 7) * 16 + 8 * (g & 1) + 4 * (n >> 3);
  if (threadIdx.x < 64) *(LAS u16*)(L + L_OB + 2304 + l * 2) = (l >> 3) == (l & 7) ? 0x3F80 : 0;
  WST(rid, 100);
  const u64 wsa = (u64)p.ws;
  const unsigned ws0 = (unsigned)wsa, ws1 = (unsigned)(wsa >> 32) & 0xFFFFu;
  u16x sa, sb;
  sa[0] = ws0; sa[1] = ws1; sa[2] = WS_BYTES; sa[3] = 0x00020000u;
  sa[4] = ws0; sa[5] = ws1; sa[6] = isred ? WS_BYTES : 0u; sa[7] = 0x00020000u;
  sa[8] = OFF_AG + sel + (rid >> 4) * 16384 + (w & 1) * 4096;
  sa[9] = OFF_AG + sel + 8 * 16384 + (rid >> 4) * 1024 + (w & 1) * 512;
  sa[10] = agt; sa[11] = rst;
  sa[12] = OFF_RS + sel + 4 * G * 16384 + head * 128;
  sa[13] = ((w & 2) ? OFF_VX : OFF_KX) + head * 8192 + (w & 1) * 4096;
  sa[14] = L_KV + w * 4096;
  sa[15] = hf ? OFF_VM + head * 4096 + w * 1024 : OFF_KM + head * 2048 + w * 512;
  sb[0] = hf ? 131072 : 65536;
  sb[1] = OFF_FM - 4096 + (head * 2 + hf) * 64;
  sb[2] = OFF_TB + sel; sb[3] = OFF_FKV + head * 2048;
  sb[4] = p.d1; sb[5] = p.d2; sb[6] = L_KB;
  sb[7] = (isred ? 1 : 0) | (hf << 1) | (w == 3 ? 4 : 0) | (w >= 2 ? 16 : 0);
  sb[8] = (w & 1) * 4096; sb[9] = 8192 + (w & 1) * 512;
  sb[10] = asu(SCL2); sb[11] = task < 64 ? 14 : 10;
  sb[12] = OFF_DBG + (rid * 4 + w) * 16384;
  sb[13] = p.d3; sb[14] = p.d4; sb[15] = 0;
#pragma unroll
  for (int i = 0; i < 16; ++i) { sa[i] = __builtin_amdgcn_readfirstlane(sa[i]); sb[i] = __builtin_amdgcn_readfirstlane(sb[i]); }
  u16x vc;
  vc[0] = vc[1] = vc[2] = vc[3] = 0x3F803F80u; vc[4] = vc[5] = 0x3F003F00u;
  vc[6] = idx; vc[7] = aQW; vc[8] = aQR; vc[9] = aOW; vc[10] = aOR; vc[11] = vRS4; vc[12] = vRS1; vc[13] = vTB; vc[14] = aKR; vc[15] = 0;
  asm volatile(R_LOOP_ASM "L_code_end:\n"
      : "+{s[36:51]}"(sa), "+{s[52:67]}"(sb), "+{v[240:255]}"(vc)
      : "{v[0:1]}"(u2{l16, l16 + w * 2048}), "{v[238:239]}"(u2{wb, wb + 65536}),
        "{v[2:33]}"(c4(Wo[0], Wo[1])), "{v[34:65]}"(c4(Wo[2], Wo[3])), "{v[66:97]}"(c4(Wo[4], Wx[3])), "{v[98:129]}"(c4(Wx[4], Wx[5])), "{v[130:145]}"(c2(Wx[6])),
        "{a[0:31]}"(c4(Wx[7], Wx[8])), "{a[32:63]}"(c4(Wy[2], Wy[3])), "{a[64:95]}"(c4(Wy[4], Wy[5])), "{a[96:127]}"(c4(Wy[6], Wy[7])), "{a[128:159]}"(c4(Wy[8], Wv[0])),
        "{a[160:191]}"(c4(Wv[1], Wv[2])), "{a[192:223]}"(c4(Wv[3], Wv[4])), "{a[224:255]}"(c4(Wv[5], Wv[6])), [smax] "n"(SPIN_MAX)
      : R_LOOP_CLOBBERS);
}

typedef float f16v __attribute__((ext_vector_type(16)));
struct Strm { u4 r0, r1; unsigned so, st, vA, vB; };
DI unsigned rfl(unsigned v) { return __builtin_amdgcn_readfirstlane(v); }
DI unsigned ops(unsigned v) { asm volatile("" : "+s"(v)); return v; }
DI u4 mkr(u64 a, unsigned bytes) { return u4{ops((unsigned)a), ops((unsigned)(a >> 32) & 0xFFFFu), bytes, 0x00020000u}; }
DI void set_rows(Strm& s, unsigned stride, int w, int l) {
  s.st = 64 * stride;
  s.vA = (16 * w + (l >> 3)) * stride + (((l & 7) ^ ((l >> 2) & 4)) * 16);
  s.vB = s.vA + 8 * stride;
}
template <int N, int K> DI void issue1(Strm& s, unsigned lw) {
  asm volatile("s_add_u32 m0, %0, %1\n\ts_nop 0\n\tbuffer_load_dwordx4 %2, %3, %4 offen lds" ::"s"(lw), "n"(N * 16384 + (K >> 1) * 8192 + (K & 1) * 1024), "v"((K & 1) ? s.vB : s.vA), "s"((K >> 1) ? s.r1 : s.r0), "s"(s.so) : "memory", "scc");
  if (K == 3) s.so += s.st;
}
template <int N> DI void issue4(Strm& s, unsigned lw) { issue1<N, 0>(s, lw); issue1<N, 1>(s, lw); issue1<N, 2>(s, lw); issue1<N, 3>(s, lw); }
struct FA { unsigned a[3][2]; };
template <int OFF, int C> DI bf8 wfr(LAS char* L, const FA& fa) { return cat8(trd(L + (OFF & 65535), fa.a[OFF >> 16][C]), trd(L + (OFF & 65535) + 1024, fa.a[OFF >> 16][C])); }
template <int N, int ST, int M> DI bf8 frag(LAS char* L, const FA& fa) { return wfr<N * 16384 + (M >> 1) * 8192 + ST * 2048, (M & 1)>(L, fa); }
template <bool Z, int R> DI void mmw(f16v& c, bf8 w) {
  if constexpr (Z) asm volatile("v_mfma_f32_32x32x16_bf16 %0, %1, a[%c2:%c3], 0" : "=&v"(c) : "v"(w), "n"(R), "n"(R + 3));
  else asm volatile("v_mfma_f32_32x32x16_bf16 %0, %1, a[%c2:%c3], %0" : "+v"(c) : "v"(w), "n"(R), "n"(R + 3));
}
template <bool Z, int R> DI void mmx(f16v& c, bf8 w) {
  if constexpr (Z) asm volatile("v_mfma_f32_32x32x16_bf16 %0, a[%c2:%c3], %1, 0" : "=&v"(c) : "v"(w), "n"(R), "n"(R + 3));
  else asm volatile("v_mfma_f32_32x32x16_bf16 %0, a[%c2:%c3], %1, %0" : "+v"(c) : "v"(w), "n"(R), "n"(R + 3));
}
template <int I> DI void ldx(rsrc_t r, unsigned voff, unsigned soff) {
  asm volatile("buffer_load_dwordx4 a[%c0:%c1], %2, %3, %4 offen offset:%c5" ::"n"(4 * I), "n"(4 * I + 3), "v"(voff), "s"(r), "s"(soff + (I >> 2) * 4096), "n"((I & 3) * 1024) : "memory");
}
template <int I> DI void ldx_all(rsrc_t r, unsigned voff, unsigned soff) { ldx<I>(r, voff, soff); if constexpr (I < 35) ldx_all<I + 1>(r, voff, soff); }
template <int R> DI void aw4(u4 v) {
  asm volatile("v_accvgpr_write_b32 a%c4, %0\n\tv_accvgpr_write_b32 a%c5, %1\n\tv_accvgpr_write_b32 a%c6, %2\n\tv_accvgpr_write_b32 a%c7, %3" ::"v"(v.x), "v"(v.y), "v"(v.z), "v"(v.w), "n"(R), "n"(R + 1), "n"(R + 2), "n"(R + 3));
}
template <int R, int OFF> DI void tra(unsigned a) {
  asm volatile("ds_read_b64_tr_b16 a[%c1:%c2], %0 offset:%c5\n\tds_read_b64_tr_b16 a[%c3:%c4], %0 offset:%c6" ::"v"(a), "n"(R), "n"(R + 1), "n"(R + 2), "n"(R + 3), "n"(OFF), "n"(OFF + 1024) : "memory");
}
constexpr int AH = 144;
template <int R, int KS> DI void wo_ld(unsigned a) { tra<R + 4 * KS, KS * 2048>(a); if constexpr (KS < 7) wo_ld<R, KS + 1>(a); }
template <int R, int K> DI void oproj1(f16v& c, const bf8 (&ob)[16]) { mmx<K == 0, R + 4 * K>(c, ob[K]); if constexpr (K < 15) oproj1<R, K + 1>(c, ob); }
DI u4 pk8(const f16v& c, int p) { return u4{pk2(c[8 * p], c[8 * p + 1]), pk2(c[8 * p + 2], c[8 * p + 3]), pk2(c[8 * p + 4], c[8 * p + 5]), pk2(c[8 * p + 6], c[8 * p + 7])}; }
DI void pin16(f16v& a, f16v& b) { asm volatile("s_nop 7\n\ts_nop 3" : "+v"(a), "+v"(b)); }

#define STG 100
#define PUBN 3
struct WCtx { LAS char* L; FA fa; unsigned lw, l16, vfl; rsrc_t rws; };

template <int MODE, int RL, int N, int ST, int M> DI void slot(const WCtx& cx, f16v (&acc)[4], bf8 (&f)[2][4], Strm& pv, Strm& nx, unsigned xs) {
  constexpr int cur = ST & 1, nxt = cur ^ 1;
  constexpr bool Z = N == 0 && ST == 0, on = MODE < 3 || M < 2;
  bf8& fh = f[ST >> 1][2 * (ST & 1) + (M & 1)];
  if constexpr (!on) {}
  else if constexpr (MODE == 0) mmw<Z, 16 * N + 4 * ST>(acc[M], f[cur][M]);
  else if constexpr (MODE == 1) mmx<Z, 16 * N + 4 * ST>(acc[M], f[cur][M]);
  else if constexpr (MODE == 4) mmw<Z, 16 * N + 4 * ST>(acc[M], fh);
  else if constexpr (MODE == 5) mmx<Z, 16 * N + 4 * ST>(acc[M], fh);
  else mmw<Z, (N < 7 ? AH + 16 * N : 16 * (N - 7)) + 4 * ST>(acc[M], f[cur][M]);
  if constexpr (MODE >= 4) { if constexpr (on && !(N == 8 && ST >= 2)) f[(ST >> 1) ^ 1][2 * (ST & 1) + M] = frag<ST < 2 ? N : N + 1, (ST + 2) & 3, M>(cx.L, cx.fa); }
  else if constexpr (on && !(MODE == 3 && N == 8 && ST == 3)) f[nxt][M] = frag<ST < 3 ? N : (N + 1) % 9, (ST + 1) & 3, M>(cx.L, cx.fa);
  if constexpr (M == 1 && MODE < 4) {
    constexpr int R = ST == 3 ? N - 1 : N - 2;
    if constexpr (RL != 2 || R < 0) issue1<(R + 9) % 9, (ST + 1) & 3>(R < 0 ? pv : nx, cx.lw);
  }
  if constexpr (RL && M == 3 && N > 0) ldx<4 * (N - 1) + ST>(cx.rws, cx.l16, xs);
  SB();
}
template <int MODE, int RL, int N, int ST> DI void step(const WCtx& cx, f16v (&acc)[4], bf8 (&f)[2][4], Strm& pv, Strm& nx, unsigned xs) {
  slot<MODE, RL, N, ST, 0>(cx, acc, f, pv, nx, xs); slot<MODE, RL, N, ST, 1>(cx, acc, f, pv, nx, xs); slot<MODE, RL, N, ST, 2>(cx, acc, f, pv, nx, xs); slot<MODE, RL, N, ST, 3>(cx, acc, f, pv, nx, xs);
}
template <int MODE, int RL, int N> DI void chunk(const WCtx& cx, f16v (&acc)[4], bf8 (&f)[2][4], Strm& pv, Strm& nx, unsigned xs, unsigned aux) {
  if constexpr (MODE >= 4) {
    if (N == PUBN && aux) {
      asm volatile("s_waitcnt vmcnt(%0)\n\ts_barrier" ::"n"(4 * PUBN - 4) : "memory");
      st4(1u, cx.rws, cx.vfl, aux);
    } else wvm<32>();
    SB();
  }
  step<MODE, RL, N, 0>(cx, acc, f, pv, nx, xs); step<MODE, RL, N, 1>(cx, acc, f, pv, nx, xs); step<MODE, RL, N, 2>(cx, acc, f, pv, nx, xs);
  if constexpr (MODE >= 4) {}
  else if constexpr (N == 4) {
    asm volatile("s_waitcnt vmcnt(19)\n\ts_barrier" ::: "memory");
    st4(1u, cx.rws, cx.vfl, aux);
  } else asm volatile("s_waitcnt vmcnt(24)\n\ts_barrier" ::: "memory");
  SB();
  step<MODE, RL, N, 3>(cx, acc, f, pv, nx, xs);
  if constexpr (N < 8) chunk<MODE, RL, N + 1>(cx, acc, f, pv, nx, xs, aux);
}
DI void pinf(bf8 (&f)[2][4]) { asm volatile("s_nop 1" : "+{v[66:69]}"(f[0][0]), "+{v[70:73]}"(f[0][1]), "+{v[74:77]}"(f[0][2]), "+{v[78:81]}"(f[0][3])); }
template <int MODE, int RL> DI void unit(const WCtx& cx, f16v (&acc)[4], bf8 (&f)[2][4], Strm& pv, Strm& nx, unsigned xs, unsigned aux) {
  chunk<MODE, RL, 0>(cx, acc, f, pv, nx, xs, aux);
  if constexpr (MODE >= 3) asm volatile("s_nop 7\n\ts_nop 3" : "+v"(acc[0]), "+v"(acc[1]));
  else asm volatile("s_nop 7\n\ts_nop 3" : "+{v[2:17]}"(acc[0]), "+{v[18:33]}"(acc[1]), "+{v[34:49]}"(acc[2]), "+{v[50:65]}"(acc[3]));
  if constexpr (RL) { ldx<32>(cx.rws, cx.l16, xs); ldx<33>(cx.rws, cx.l16, xs); ldx<34>(cx.rws, cx.l16, xs); ldx<35>(cx.rws, cx.l16, xs); }
  if constexpr (RL == 2) pv.so -= 2 * pv.st;
  else if constexpr (MODE < 4) pv = nx;
}
DI float geglu(float g, float u) {
  const float c0 = 2.0f * 0.7978845608028654f * 1.4426950408889634f, c1 = c0 * 0.044715f;
  const float z = g * __builtin_fmaf(g * g, c1, c0);
  return g * u * __builtin_amdgcn_rcpf(1.0f + __builtin_amdgcn_exp2f(-z));
}
#define TLBW 2
#define PROBES 8
#define PF_T0 1000
#define PF_SL 0
DI void until_s(unsigned t) { for (int it = 0; it < 4096 && (int)(rtime() - t) < 0; ++it) asm volatile("s_sleep 8"); }
DI void role_p1(const Prm& p, const int nid) {
  const int w = rfl(threadIdx.x >> 6), l = threadIdx.x & 63;
  const rsrc_t rq = mk_rsrc(p.Wq, D_ * HE_ * 2), rk = mk_rsrc(p.Wk, D_ * HE_ * 2), rv = mk_rsrc(p.Wv, D_ * HE_ * 2);
  const unsigned vo = (32 * (nid & 1) + 8 * w + (l >> 4)) * 8192 + (nid >> 1) * 256 + (l & 15) * 16;
#pragma unroll
  for (int i = 3; i < 27; ++i) {
    const LdE e = ld_list(i);
#pragma unroll
    for (int k = 0; k < 2; ++k) dma_i(w * 1024, 147456, vo, e.m == 0 ? rq : (e.m == 1 ? rk : rv), (e.ks * 64 + 4 * k) * 8192);
  }
}
DI void role_p2(const Prm& p, const int nid, const unsigned t0) {
  const int w = rfl(threadIdx.x >> 6), l = threadIdx.x & 63;
  until(t0 + PF_T0);
  for (int m = 0; m < 3; ++m) {
    const rsrc_t r = mk_rsrc(m == 0 ? p.wg : (m == 1 ? p.wu : p.wd), D_ * F_ * 2);
    for (int i = 0; i < 21; ++i) {
      dma(147456 + w * 1024, l * 16, r, nid * 82944 + min(4 * i + w, 80) * 1024);
      asm volatile("s_sleep %0" ::"n"(PF_SL));
    }
  }
}
#define WSTS(i)
DI float dsum8(float v) {
  v += asf(__builtin_amdgcn_update_dpp(0, asu(v), 0xB1, 0xF, 0xF, true));
  v += asf(__builtin_amdgcn_update_dpp(0, asu(v), 0x4E, 0xF, 0xF, true));
  return v + asf(__builtin_amdgcn_update_dpp(0, asu(v), 0x141, 0xF, 0xF, true));
}
DI unsigned touch_page(const Prm& p, unsigned k) {
  u64 a = (u64)p.ws + ((u64)min(k, 63u) << 21);
  const unsigned t = (k - 64) >> 2, o = (k & 3) << 21;
  const u16* const b[10] = {p.Wk, p.Wv, p.x, p.Wq, p.Wo, p.wg, p.wu, p.wd, p.out, p.mem0};
#pragma unroll
  for (unsigned i = 0; i < 10; ++i) a = t == i ? (u64)b[i] + min(o, (i == 2 || i == 8 ? S_ * D_ * 2u : (i < 5 ? D_ * HE_ * 2u : (i < 8 ? D_ * F_ * 2u : 64u))) - 4u) : a;
  unsigned d;
  asm volatile("global_load_dword %0, %1, off" : "=&v"(d) : "v"(a) : "memory");
  return d;
}
#define DSG 61
constexpr unsigned DSEG = DSG;
static_assert(DSEG >= 60 && PART_SLOT == 4 * 9216, "reset");
DI void reset_share(rsrc_t rws, unsigned id) {
  const unsigned now = rtime();
  for (unsigned o = id * 16; o < CLR_END; o += 262144) {
    const unsigned v = o - OFF_TB < 16384u ? now : ~0u;
    st16(u4{v, v, v, v}, rws, o - OFF_AG < AG_SLOT || o - OFF_PRB < 16384u ? 0x80000000u : o, 0);
  }
}
DI void reset_last(rsrc_t rws, unsigned head, unsigned kv, unsigned id) {
  const u4 v = {~0u, ~0u, ~0u, ~0u};
  for (unsigned s = DSEG; s < 64; ++s) {
    st16(v, rws, kv || id < 128 ? id * 16 : 0x80000000u, kv ? OFF_VM + (s * 32 + head) * 4096 : OFF_KM + (s * 32 + head) * 2048);
#pragma unroll
    for (unsigned i = 0; i < 3; ++i) st16(v, rws, i < 2 || id < 64 ? id * 16 + i * 4096 : 0x80000000u, OFF_PART + (s * NPART + 8 + (head >> 1)) * PART_SLOT + ((head & 1) * 2 + kv) * 9216);
  }
  wvm<0>();
}
DI void xn_store(const u4 (&o)[9], rsrc_t rws, unsigned vo, int blk) {
#pragma unroll
  for (int i = 0; i < 9; ++i) st16(o[i], rws, vo, OFF_XN + (blk >> 1) * 36864 + i * 4096);
}
DI u2 tokens(const Prm& p, rsrc_t rws, int wid, int blk, bool crit, int w, int l, unsigned vfl, u4 (&ot)[9], unsigned vo) {
  const int j = l & 7, tk = 8 * w + (l >> 3);
  const rsrc_t rx = mk_rsrc(p.x, S_ * D_ * 2), rm = mk_rsrc(p.mem0, 8 * D_ * 2);
  u4 raw[9], m0[3];
#pragma unroll
  for (int i = 0; i < 9; ++i) raw[i] = ld16p(rx, (16 * blk + tk) * 1152 + j * 16, i * 128);
  const u2 gen = ld8(rws, 0, OFF_GEN);
  SB();
  const unsigned id = (wid >> 2) * 128 + w * 64 + l;
#pragma unroll
  for (int r = 0; r < 3; ++r) {
    const unsigned task = r < 2 ? (id >> 3) & 63 : 64 + ((id >> 3) & 7);
    m0[r] = ld16p(rm, crit ? (4 * (task & 1) + 2 * (j >> 2) + (j & 1)) * 1152 + 32 * (task >> 1) + 16 * ((j >> 1) & 1) : 0x80000000u, 0);
  }
  SB();
  WSTS(56);
  SB();
  f2 v[36], s = {0.f, 0.f};
#pragma unroll
  for (int i = 0; i < 36; ++i) { v[i] = f2{asf(raw[i >> 2][i & 3] << 16), asf(raw[i >> 2][i & 3] & 0xFFFF0000u)}; s += v[i]; }
  const float mean = dsum8(s.x + s.y) * (1.0f / D_);
  asm volatile("" :: "v"(mean));
  WSTS(57);
  f2 q = {0.f, 0.f};
#pragma unroll
  for (int i = 0; i < 36; ++i) { const f2 d = v[i] - mean; q += d * d; }
  const float m = rbf(mean), var = rbf(dsum8(q.x + q.y) * (1.0f / D_));
  const float inv = 1.0f / rbf(sqrtf(rbf(var + 1e-5f)));
#pragma unroll
  for (int i = 0; i < 9; ++i) {
    u4 o;
#pragma unroll
    for (int e = 0; e < 4; ++e) {
      const f2 d = v[4 * i + e] - m;
      const unsigned b = pk2(d.x, d.y);
      const f2 n = f2{asf(b << 16), asf(b & 0xFFFF0000u)} * inv;
      o[e] = pk2(n.x, n.y);
    }
    const unsigned a = __builtin_amdgcn_update_dpp(0, (j & 1) ? o.x : o.z, 0xB1, 0xF, 0xF, true), b = __builtin_amdgcn_update_dpp(0, (j & 1) ? o.y : o.w, 0xB1, 0xF, 0xF, true);
    ot[i] = (j & 1) ? u4{a, b, o.z, o.w} : u4{o.x, o.y, a, b};
  }
  const u2 tag = tag_of(gen);
  xn_store(ot, rws, vo, blk);
  if (crit) {
#pragma unroll
    for (int r = 0; r < 3; ++r) st16(m0[r], rws, r < 2 || id < 1024 ? id * 16 : 0x80000000u, OFF_AG + r * 65536);
    wvm<0>();
    st8(tag, rws, l == 0 ? 0u : 0x80000000u, OFF_FX + blk * 64 + w * 8);
    bar_nowait();
  }
  return tag;
}
DI void probe(rsrc_t r, int l, u2 tag) {
  st16(u4{0u, 0u, 0u, 0u}, r, OFF_PRB + (l >> 3) * 1024 + (l & 7) * 16, 0);
  st16(u4{0u, 0u, 0u, 0u}, r, OFF_PRB + (l >> 3) * 1024 + (l & 7) * 16, 8192);
  unsigned best[2] = {~0u, ~0u};
#pragma unroll
  for (int i = 0; i < PROBES; ++i) {
    unsigned long long t0, t1;
    unsigned v;
    asm volatile("s_waitcnt vmcnt(0)\n\ts_memtime %0\n\ts_waitcnt lgkmcnt(0)\n\tbuffer_load_dword %2, %3, %4, 0 offen sc1\n\ts_waitcnt vmcnt(0)\n\ts_memtime %1\n\ts_waitcnt lgkmcnt(0)"
                 : "=&s"(t0), "=&s"(t1), "=&v"(v) : "v"(OFF_PRB + (i & 1) * 8192 + (i >> 1) * 1024), "s"(r) : "memory");
    best[i & 1] = min(best[i & 1], (unsigned)(t1 - t0));
  }
  st8(u2{tag.x, tag.y ^ (best[0] <= best[1] ? 0u : 8192u)}, r, l == 0 ? 0u : 0x80000000u, OFF_SEL);
}
#define WO_T0 1500
#define WO_D 100
#define NSEG 32
#define NT0 3500
#define RPER 320
template <int N> struct IC { constexpr int operator()() const { return N; } };
#define DDEL 25
#define DLAG 2
#define NPER 1000
DI void role_w(const Prm& p, LAS char* L, const int wid) {
  const int w = rfl(threadIdx.x >> 6), l = threadIdx.x & 63;
  const int c = wid >> 4, mt = wid & 15;
  const bool near = wid >= 128;
  const int hp = mt, sd = near ? 4 : 8, sa = near ? c - 8 : NSEG + c, se = near ? NSEG : 64;
  const int t = l & 31, h = l >> 5;
  WCtx cx;
  cx.L = L; cx.lw = w * 2048; cx.rws = mk_rsrc(p.ws, WS_BYTES); cx.l16 = l * 16;
  const rsrc_t rws = cx.rws;
  {
    const unsigned q = (l >> 2) & 3, a0 = (4 * h + q) * 128 + (q >> 1) * 64 + ((l >> 4) & 1) * 32 + (l & 3) * 8;
#pragma unroll
    for (int b = 0; b < 3; ++b) { cx.fa.a[b][0] = opq(a0 + b * 65536); cx.fa.a[b][1] = opq((a0 ^ 64) + b * 65536); }
  }
  constexpr unsigned WB = D_ * HE_ * 2, GB = D_ * F_ * 2;
  const unsigned vfl = threadIdx.x == 0 ? 0u : 0x80000000u;
  cx.vfl = 0x80000000u;
  const unsigned vfn = (128 + l) * 64 + (w & 1) * 16;
  auto uid = [&](int it) {
    const int kv = (wid >> 1) + 128 * (2 * (it < 3 ? it : it - 1) + (wid & 1)), q = 64 + (wid >> 2) + 128 * (4 * max(it - 8, 0) + (wid & 3));
    return it == 12 ? 128 * mt : (it == 3 || it > 8 ? q : kv);
  };
  auto mkp = [&](Strm& s, int u) {
    const u64 W = (u64)(u < 64 ? ((u & 1) ? p.Wv : p.Wk) : p.Wq);
    s.r0 = mkr(W, WB); s.r1 = mkr(W + 128, WB - 128); s.so = (u < 64 ? (u >> 1) : u - 64) * 256;
  };
  auto mko = [&](Strm& s, int u) {
    s.r0 = mkr((u64)p.Wo, WB); s.r1 = mkr((u64)p.Wo + 64 * 1152, WB - 64 * 1152); s.so = (256 * hp + 128 * u) * 1152;
    set_rows(s, 1152, w, l);
    s.st = 128;
  };
  WST(wid + 64, 0);
  Strm nx, pv;
  bf8 f[2][4];
  const unsigned tn = rtime();
  unsigned tp0 = 0, tp1 = 0;
  if (w == 3) tp0 = touch_page(p, near ? (64 + hp + 16 * (l & 7)) & 127 : (64 + (wid & 31) + 32 * (l & 3)) & 127);
  if (near) {
    u2 gen;
    asm volatile("buffer_load_dwordx2 %0, off, %1, %2 sc1" : "=v"(gen) : "s"(rws), "s"(OFF_GEN) : "memory");
    reset_share(rws, (wid - 128) * 256 + threadIdx.x);
    const bool prb = wid == 128 && w == 0;
    asm volatile("" ::: "memory");
    if (prb) wvm<0>();
    else { role_p1(p, wid - 128); wvm<48>(); }
    asm volatile("" : "+v"(gen)::"memory");
    const u2 tag = tag_of(gen);
    st8(tag, rws, l == 0 ? 0u : 0x80000000u, OFF_FX + wid * 64 + w * 8);
    if (prb) { probe(rws, l, tag); role_p1(p, 0); }
    role_p2(p, wid - 128, tn);
    until_s(tn + WO_T0 + hp * WO_D);
    wait_fx(rws, vfn, tag);
    asm volatile("" ::"v"(tp0), "v"(tp1));
    mko(pv, 0);
    issue4<0>(pv, cx.lw); issue4<1>(pv, cx.lw); issue4<2>(pv, cx.lw); issue4<3>(pv, cx.lw); issue4<4>(pv, cx.lw);
    issue4<5>(pv, cx.lw); issue4<6>(pv, cx.lw); issue4<7>(pv, cx.lw); issue4<8>(pv, cx.lw);
    wvm<0>();
  } else {
    const int u0 = wid >> 1, pr = wid & 1;
    mkp(pv, u0);
    set_rows(pv, 8192, w, l);
    nx = pv;
    u2 tag;
    const bool crit = (wid & 3) == 0;
    const int blk = crit ? (wid >> 3) + 4 * (wid & 4) : 31 + wid - (wid >> 2);
    const unsigned xh = OFF_XN + (2 * pr + (w >> 1)) * 36864;
    if (w >= 2) {
      tag = call_tag(rws);
      if (crit) bar_nowait();
      Strm pa = pv, po = pv;
      set_rows(po, 8192, w - 2, l);
      const unsigned lo = cx.lw - 4096;
#define IS2(N) issue4<N>(pa, cx.lw); issue4<N>(po, lo);
      IS2(0) IS2(1) IS2(2) IS2(3) IS2(4) IS2(5) IS2(6) IS2(7) IS2(8)
#undef IS2
      bar_nowait();
      ldx_all<0>(rws, cx.l16, xh);
      wvm<0>();
    } else {
      u4 ot[9];
      const unsigned vo = (16 * (blk & 1) + 8 * w + (l >> 3) + 32 * (l & 1)) * 16 + ((l & 7) >> 1) * 1024;
      tag = tokens(p, rws, wid, blk, crit, w, l, threadIdx.x == 0 ? 0u : 0x80000000u, ot, vo);
      WST(wid + 64, 62);
      if (w == 0) wait_fx(rws, (l & 15) * 64, tag);
      bar_nowait();
      WST(wid + 64, 63);
      ldx_all<0>(rws, cx.l16, xh);
      wvm<0>();
    }
    pv.so += 7 * pv.st;
    bar();
    asm volatile("" ::"v"(tp0), "v"(tp1));
    WST(wid + 64, 1);
    unsigned pend = 0;
    const unsigned vK = ((t >> 3) & 1) * 4096 + ((t & 7) + 8 * (t >> 4) + 16 * h) * 16, vQ = (t >> 4) * 4096 + ((t & 15) + 16 * h) * 16, vV = (t >> 4) * 1024 + ((t & 15) + 16 * h) * 16;
    int cu = uid(1);
    {
      WCtx ch = cx;
#pragma unroll
      for (int b = 0; b < 3; ++b) { ch.fa.a[b][0] = opq(cx.fa.a[b][0] + (w & 1) * 8192); ch.fa.a[b][1] = opq(cx.fa.a[b][1] + (w & 1) * 8192); }
      const bool isv = u0 & 1;
      const unsigned vo = isv ? vV : vK, cs = isv ? 2048 : 1024;
      for (int hb = 0; hb < 2; ++hb) {
        f16v acc[4];
        u4 fc;
        const unsigned vfc = hb ? (32 * w + (l & 31)) * 64 : (w < 2 ? vfn : (16 + (l & 15)) * 64);
        asm volatile("buffer_load_dwordx4 %0, %1, %2, %3 offen sc1" : "=v"(fc) : "v"(vfc), "s"(rws), "s"(OFF_FX) : "memory");
        const unsigned xs = hb ? OFF_XN + (4 * (cu >> 7) + w) * 36864 : xh + 4 * 36864;
        const unsigned so = (isv ? OFF_VX : OFF_KX) + ((4 * hb + 2 * pr + (w >> 1)) * 32 + (u0 >> 1)) * 8192 + (w & 1) * 2 * cs;
        f[0][0] = frag<0, 0, 0>(L, ch.fa); f[0][1] = frag<0, 0, 1>(L, ch.fa); f[0][2] = frag<0, 1, 0>(L, ch.fa); f[0][3] = frag<0, 1, 1>(L, ch.fa);
        if (isv) unit<5, 1>(ch, acc, f, pv, nx, xs, pend);
        else unit<4, 1>(ch, acc, f, pv, nx, xs, pend);
        if (hb == 0) WST(wid + 64, 60);
        if (hb < pr) until(rtime() + STG);
#pragma unroll
        for (int i = 0; i < 4; ++i) st16(pk8(acc[i >> 1], i & 1), rws, vo + (i & 1) * 512, so + (i >> 1) * cs);
        asm volatile("s_waitcnt vmcnt(32)" : "+v"(fc)::"memory");
        st8(tag, rws, w < 2 && l == 0 ? 0u : 0x80000000u, OFF_FX + blk * 64 + w * 8);
        if (!tagged(fc, tag)) wait_fx(rws, vfc, tag);
        bar_nowait();
        ch.vfl = vfl;
        pend = OFF_FKV + (((u0 >> 1) * 16 + hb) * 2 + (u0 & 1)) * 64 + 4 * pr;
        SB();
        if (hb) WST(wid + 64, 2);
      }
      cx.vfl = vfl;
    }
    f[0][0] = frag<0, 0, 0>(L, cx.fa); f[0][1] = frag<0, 0, 1>(L, cx.fa); f[0][2] = frag<0, 0, 2>(L, cx.fa); f[0][3] = frag<0, 0, 3>(L, cx.fa);
    const unsigned vf2 = threadIdx.x < 2 ? threadIdx.x * 4 : 0x80000000u;
    for (int it = 1; it < 12; ++it) {
      f16v acc[4];
      const int u = cu & 127, m = cu >> 7, head = u < 64 ? (u >> 1) : u - 64;
      const bool isv = u < 64 && (u & 1);
      cu = uid(it + 1);
      if (it < 11) mkp(nx, cu & 127);
      else {
        nx.r0 = mkr((u64)p.wg, GB); nx.r1 = mkr((u64)p.wu, GB); nx.so = 576 * 2 * c;
        set_rows(nx, 9216, w, l);
      }
      const unsigned xs = OFF_XN + (4 * (cu >> 7) + w) * 36864;
      const unsigned so = (u < 64 ? (isv ? OFF_VX : OFF_KX) : OFF_QX) + ((4 * m + w) * 32 + head) * 8192;
      const unsigned vo = isv ? vV : (u < 64 ? vK : vQ), cs = isv ? 2048 : 1024;
      pinf(f);
      if ((cu & 127) != u) {
        if (isv) unit<1, 1>(cx, acc, f, pv, nx, xs, pend);
        else unit<0, 1>(cx, acc, f, pv, nx, xs, pend);
      } else if (isv) unit<1, 2>(cx, acc, f, pv, nx, xs, pend);
      else unit<0, 2>(cx, acc, f, pv, nx, xs, pend);
#pragma unroll
      for (int i = 0; i < 8; ++i) st16(pk8(acc[i >> 1], i & 1), rws, vo + (i & 1) * 512, so + (i >> 1) * cs);
      if (m == 15 && u < 64) reset_last(rws, head, u & 1, threadIdx.x);
      cx.vfl = vf2;
      pend = u < 64 ? OFF_FKV + ((head * 16 + m) * 2 + (u & 1)) * 64 : OFF_FQ + (head * 16 + m) * 64;
      SB();
      WST(wid + 64, 2 + it);
    }
    const unsigned park = opq(147456 + w * 4096 + cx.l16);
    for (int u = 0; u < 9; ++u) {
      f16v acc[4];
      nx.so = 576 * 2 * c + 128 * (u + 1);
      if (u == 8) {
        nx.r0 = mkr((u64)p.wd, GB); nx.r1 = mkr((u64)p.wd + 128, GB - 128); nx.so = 576 * 1152 * c;
        set_rows(nx, 1152, w, l);
      }
      pinf(f);
      unit<0, 0>(cx, acc, f, pv, nx, 0, pend);
      cx.vfl = 0x80000000u;
      u4 hv[4];
#pragma unroll
      for (int q4 = 0; q4 < 4; ++q4) {
        u4 v;
#pragma unroll
        for (int e = 0; e < 4; ++e) { const int r = 8 * (q4 & 1) + 2 * e; v[e] = pk2(geglu(acc[q4 >> 1][r], acc[2 + (q4 >> 1)][r]), geglu(acc[q4 >> 1][r + 1], acc[2 + (q4 >> 1)][r + 1])); }
        hv[q4] = v;
      }
#define HSET(R) aw4<R>(hv[0]); aw4<R + 4>(hv[1]); aw4<R + 8>(hv[2]); aw4<R + 12>(hv[3]); break;
      switch (u) {
        case 0: HSET(AH) case 1: HSET(AH + 16) case 2: HSET(AH + 32) case 3: HSET(AH + 48) case 4: HSET(AH + 64) case 5: HSET(AH + 80) case 6: HSET(AH + 96)
        case 7:
#pragma unroll
          for (int i = 0; i < 4; ++i) *(LAS u4*)(L + park + i * 1024) = hv[i];
          break;
        default:
          asm volatile("ds_read_b128 a[0:3], %0\n\tds_read_b128 a[4:7], %0 offset:1024\n\tds_read_b128 a[8:11], %0 offset:2048\n\tds_read_b128 a[12:15], %0 offset:3072" ::"v"(park) : "memory");
          HSET(16)
      }
#undef HSET
      SB();
      WST(wid + 64, 14 + u);
    }
    asm volatile("s_waitcnt lgkmcnt(0)" ::: "memory");
    const unsigned so = OFF_PART + ((4 * mt + w) * NPART + c) * PART_SLOT;
    for (int u = 0; u < 4; ++u) {
      f16v acc[4];
      nx.so = 576 * 1152 * c + 256 * (u + 1);
      nx.r1.z = u < 3 ? GB - 128 : 0u;
      pinf(f);
      unit<2, 0>(cx, acc, f, pv, nx, 0, 0);
#pragma unroll
      for (int i = 0; i < 8; ++i) st16(pk8(acc[i >> 1], i & 1), rws, cx.l16 + (i & 3) * 1024, so + u * 8192 + (i >> 2) * 4096);
      SB();
      WST(wid + 64, 23 + u);
    }
    f16v acc[4];
    mko(nx, 0);
    unit<3, 0>(cx, acc, f, pv, nx, 0, 0);
#pragma unroll
    for (int i = 0; i < 4; ++i) st16(pk8(acc[i >> 1], i & 1), rws, cx.l16 + i * 1024, so + 32768);
    WST(wid + 64, 27);
    bar();
    issue1<7, 1>(pv, cx.lw); issue1<7, 2>(pv, cx.lw); issue1<7, 3>(pv, cx.lw); issue4<8>(pv, cx.lw);
    wvm<0>();
    bar_nowait();
    st4(1u, rws, vfl, OFF_FD + (mt * 16 + c) * 64);
    WST(wid + 64, 30);
  }
  f16v acc[4];
  bf8 Wo4[16];
  const int cb0 = w < 2 ? 5 * w : 10 + 4 * (w - 2);
  auto wo_addr = [&](int lb) { const int cbg = min(cb0 + lb, 17); return opq((cx.fa.a[0][0] ^ ((cbg & 1) * 64)) + (cbg >> 1) * 16384); };
#pragma unroll
  for (int U = 0; U < 2; ++U) {
    bar();
    if (U == 0) { wo_ld<0, 0>(wo_addr(0)); wo_ld<64, 0>(wo_addr(1)); wo_ld<128, 0>(wo_addr(2)); wo_ld<192, 0>(wo_addr(3)); }
    else { wo_ld<32, 0>(wo_addr(0)); wo_ld<96, 0>(wo_addr(1)); wo_ld<160, 0>(wo_addr(2)); wo_ld<224, 0>(wo_addr(3)); }
    const unsigned a = wo_addr(4);
#pragma unroll
    for (int ks = 0; ks < 8; ++ks) Wo4[8 * U + ks] = cat8(trd(L + ks * 2048, a), trd(L + ks * 2048 + 1024, a));
    bar();
    if (U == 0) {
      mko(nx, 1);
      issue4<0>(nx, cx.lw); issue4<1>(nx, cx.lw); issue4<2>(nx, cx.lw); issue4<3>(nx, cx.lw); issue4<4>(nx, cx.lw);
      issue4<5>(nx, cx.lw); issue4<6>(nx, cx.lw); issue4<7>(nx, cx.lw); issue4<8>(nx, cx.lw);
      wvm<0>();
    }
  }
  const int n = l & 15, g = l >> 4, hh = w >> 1, T = w & 1, head = 2 * hp + hh;
  const unsigned l16 = l * 16;
  const int dq = 16 * T + n - (16 * (g >> 1) + 4 * (g & 1));
  const unsigned aOW = hh * 8192 + (16 * T + n + 32 * (g & 1)) * 16 + (g >> 1) * 8;
  const int piece = (3 - w) * 16 + hp, wp = piece < 20 ? piece / 10 : (piece - 4) / 8;
  const unsigned fb = l < 2 ? OFF_FKV + head * 2048 + l * 64 : (l == 2 ? OFF_FQ + head * 1024 : (l < 16 ? OFF_FM + head * 128 + (l & 1) * 64 : (l < 32 ? OFF_FP + (min(wp, 3) * 16 + l - 16) * 64 : OFF_FD + (l & 7) * 64)));
  const unsigned fs = l < 2 ? 128 : (l == 2 ? 64 : (l < 32 ? 4096 : 1024)), fh = (l < 3 || l >= 32) ? 2 : 0;
  const unsigned fq = l < 2 ? 2 : 0;
  const int cp = min(c, 3);
  const unsigned fz = (l & 48) == 16 ? min((piece >> 1) - (wp < 2 ? 5 * wp : 2 + 4 * wp), 3) * 4 : 0;
  auto look = [&](int s, int r) { const unsigned x = min(l < 16 ? s : r, 63); return ld4(rws, fb + (x >> fh) * fs + (x & fq) * 2 + (x == 63 ? fz : 0), 0); };
  auto await = [&](int s, int r, u64 mask) {
    for (int sp = 0; sp < SPIN_MAX; ++sp) {
      if ((__ballot(look(s, r) == ~0u) & mask) == 0ull) break;
      asm volatile("s_sleep 4" ::: "memory");
      if (near) asm volatile("s_sleep 32" ::: "memory");
    }
    asm volatile("" ::: "memory");
  };
  const rsrc_t rx = mk_rsrc(p.x, S_ * D_ * 2), ro = mk_rsrc(p.out, S_ * D_ * 2);
  unsigned ob = 0;
  int nr = piece < 36 ? sa : 72;
  u64 miss = ~0ull;
  if (!near) for (int sp = 0; sp < SPIN_MAX; ++sp) {
    if (__ballot(ld4(rws, (l >> 1) * 2048 + (l & 1) * 64, OFF_FKV + 15 * 128) == ~0u) == 0ull) break;
    asm volatile("s_sleep 4" ::: "memory");
  }
  WST(wid + 64, 31);
  for (int s = sa;; s += sd) {
    const bool ready = !near && s < se && (miss & 0xFFFFull) == 0ull;
    const unsigned fp = OFF_FP + ((s - sd) * 64 + w * 16 + hp) * 64 + (s - sd == 63 ? cp * 4 : 0);
    if (!ready && s != sa) {
      wvm<0>();
      if (l == 0) st4(1u, rws, 0, fp);
    }
    auto psum = [&](auto N, int r, int first) {
      unsigned sp = OFF_PART + (r * NPART + first) * PART_SLOT + piece * 1024;
      asm volatile("" : "+s"(sp));
      u4 d[N()], o;
#pragma unroll
      for (int i = 0; i < N(); ++i) d[i] = ld16(rws, l16, sp + i * PART_SLOT);
      float a[8];
#pragma unroll
      for (int e2 = 0; e2 < 8; ++e2) a[e2] = 0.f;
#pragma unroll
      for (int i = 0; i < N(); ++i)
#pragma unroll
        for (int e2 = 0; e2 < 4; ++e2) { a[2 * e2] += asf(d[i][e2] << 16); a[2 * e2 + 1] += asf(d[i][e2] & 0xFFFF0000u); }
#pragma unroll
      for (int e2 = 0; e2 < 2; ++e2) {
        auto ra = __builtin_amdgcn_permlane32_swap(pk2(a[2 * e2], a[2 * e2 + 1]), pk2(a[4 + 2 * e2], a[5 + 2 * e2]), false, false);
        o[e2] = ra[0]; o[2 + e2] = ra[1];
      }
      return o;
    };
    auto add8 = [&](u4 a, u4 b) {
      u4 o;
#pragma unroll
      for (int e2 = 0; e2 < 4; ++e2) o[e2] = pk2(asf(a[e2] << 16) + asf(b[e2] << 16), asf(a[e2] & 0xFFFF0000u) + asf(b[e2] & 0xFFFF0000u));
      return o;
    };
    if (near) {
      for (; nr + (s < se ? 2 * sd : sd) <= s; nr += sd) {
        const u4 xv = ld16p(rx, (nr * 32 + t) * 1152 + piece * 32 + h * 16, 0);
        await(0, nr, 0xFFFF0000ull);
        *(LAS u4*)(L + 65536 + (nr >> 2) * 4096 + w * 1024 + l16) = add8(xv, psum(IC<16>(), nr, 8));
      }
      if (s >= se && piece < 36) {
        unsigned t1 = 0;
        for (int r = sa; r < se; r += sd) {
          await(0, r, ~0xFFFFFFFFull);
          if (r == sa) t1 = rtime() + hp * (RPER / 16);
          until_s(t1 + (r >> 2) * RPER);
          const u4 ov = add8(*(LAS u4*)(L + 65536 + (r >> 2) * 4096 + w * 1024 + l16), psum(IC<8>(), r, 0));
          __builtin_amdgcn_raw_buffer_store_b128(ov, ro, (r * 32 + t) * 1152 + piece * 32 + h * 16, 0, 0);
          if (l == 0) { WSTL(wid + 64, 66 + (r & ~3) + (w == 3)); }
        }
      }
    } else
    for (; nr + (ready ? 2 * sd : sd) <= s; nr += sd) {
      const unsigned vo = (nr * 32 + t) * 1152 + piece * 32 + h * 16;
      const u4 xv = ld16p(rx, vo, 0);
      bool known = nr < (int)DSEG || (miss >> 16) == 0ull;
      if (miss >> (known ? 16 : 32)) await(0, nr, known ? ~0xFFFFull : ~0xFFFFFFFFull);
      miss = ~0ull;
      unsigned sp = OFF_PART + nr * NPART * PART_SLOT + piece * 1024;
      u4 d[NPART];
      for (int it = 0; it < SPIN_MAX; ++it) {
        asm volatile("" : "+s"(sp)::"memory");
        const unsigned fl = look(0, nr);
#pragma unroll
        for (int i = 0; i < NPART; ++i) d[i] = ld16(rws, l16, sp + i * PART_SLOT);
        unsigned m = 0;
#pragma unroll
        for (int i = 8; i < NPART; ++i) m = max3u(max3u(m, d[i].x, d[i].y), d[i].z, d[i].w);
        asm volatile("" : "+v"(m));
        if (known || __ballot(m == ~0u) == 0ull) break;
        known = (__ballot(fl == ~0u) >> 16) == 0ull;
      }
      float a[8], b[8];
#pragma unroll
      for (int e2 = 0; e2 < 8; ++e2) { a[e2] = 0.f; b[e2] = 0.f; }
#pragma unroll
      for (int i = 0; i < NPART; ++i)
#pragma unroll
        for (int e2 = 0; e2 < 4; ++e2) {
          const float lo = asf(d[i][e2] << 16), hi = asf(d[i][e2] & 0xFFFF0000u);
          if (i < 8) { b[2 * e2] += lo; b[2 * e2 + 1] += hi; } else { a[2 * e2] += lo; a[2 * e2 + 1] += hi; }
        }
      u4 pa, pb4;
#pragma unroll
      for (int e2 = 0; e2 < 2; ++e2) {
        auto ra = __builtin_amdgcn_permlane32_swap(pk2(a[2 * e2], a[2 * e2 + 1]), pk2(a[4 + 2 * e2], a[5 + 2 * e2]), false, false);
        auto rb = __builtin_amdgcn_permlane32_swap(pk2(b[2 * e2], b[2 * e2 + 1]), pk2(b[4 + 2 * e2], b[5 + 2 * e2]), false, false);
        pa[e2] = ra[0]; pa[2 + e2] = ra[1]; pb4[e2] = rb[0]; pb4[2 + e2] = rb[1];
      }
      __builtin_amdgcn_raw_buffer_store_b128(add8(add8(xv, pa), pb4), ro, vo, 0, 0);
      if (l == 0) { WSTL(wid + 64, 66 + ((nr - NSEG) >> 3) * 4 + (w == 3)); }
    }
    if (s >= se) {
      if (c > 2 || s == 71) break;
      s = 63;
    } else if (near) until_s(tn + NT0 + (s >> 2) * NPER + hp * (NPER / 16));
    if (!ready) await(s, 0, 7ull);
    const unsigned sh = (s * 32 + head) * 8192;
    bf8 qB[4], kx[8], vx[8];
#pragma unroll
    for (int i = 0; i < 4; ++i) qB[i] = __builtin_bit_cast(bf8, ld16(rws, l16 + i * 1024, OFF_QX + sh + T * 4096));
#pragma unroll
    for (int i = 0; i < 8; ++i) kx[i] = __builtin_bit_cast(bf8, ld16(rws, l16 + (i & 3) * 1024, OFF_KX + sh + (i >> 2) * 4096));
#pragma unroll
    for (int i = 0; i < 8; ++i) vx[i] = __builtin_bit_cast(bf8, ld16(rws, l16 + (i & 3) * 1024, OFF_VX + sh + (i >> 2) * 4096));
    bool known = ready || s < (int)DSEG;
    if (s >= (int)DSEG) {
      if (w == 0) for (int sp = 0; sp < SPIN_MAX; ++sp) {
        if (rfl(ld4(rws, 0, OFF_FM + ((s - DLAG) * 64 + 4 * hp + (c & 3)) * 64)) != -1) break;
        asm volatile("s_sleep 4" ::: "memory");
      }
      bar_nowait();
      if (!known) asm volatile("s_sleep %0" ::"n"(DDEL) : "memory");
    } else if (!ready) await(s, 0, 0xFFFFull);
    WST1(wid + 64, 64 + (near ? s & ~3 : ((s - NSEG + (c < 3 ? 1 : 0)) >> 3) * 4));
    bf8 km[4];
    u4 vm[4];
    for (int it = 0; it < SPIN_MAX; ++it) {
      const unsigned fl = look(s, 0);
      u4 kr[4];
#pragma unroll
      for (int i = 0; i < 4; ++i) { kr[i] = ld16(rws, g * 128 + (2 * (n >> 2) + (n & 1)) * 16 + i * 512, OFF_KM + (s * 32 + head) * 2048); km[i] = __builtin_bit_cast(bf8, kr[i]); }
#pragma unroll
      for (int i = 0; i < 4; ++i) vm[i] = ld16(rws, l16 + i * 1024, OFF_VM + (s * 32 + head) * 4096);
      unsigned m = 0;
#pragma unroll
      for (int i = 0; i < 4; ++i) m = max3u(max3u(max3u(max3u(m, kr[i].x, kr[i].y), kr[i].z, kr[i].w), vm[i].x, vm[i].y), vm[i].z, vm[i].w);
      asm volatile("" : "+v"(m));
      if (known || __ballot(m == ~0u) == 0ull) break;
      known = (__ballot(fl == ~0u) & 0xFFFFull) == 0ull;
      asm volatile("" ::: "memory");
    }
    f4 s0 = amf32z(kx[0], qB[0]), s1 = amf32z(kx[4], qB[0]);
#pragma unroll
    for (int i = 1; i < 4; ++i) { amf32(s0, kx[i], qB[i]); amf32(s1, kx[4 + i], qB[i]); }
    if (ready && l == 0) st4(1u, rws, 0, fp);
    f4 sm = amf32z(km[0], qB[0]);
#pragma unroll
    for (int i = 1; i < 4; ++i) amf32(sm, km[i], qB[i]);
    pin<7>(sm);
    asm volatile("" : "+v"(s0), "+v"(s1));
    float mx = fmaxf(sm[0], sm[1]);
#pragma unroll
    for (int b = 0; b < 4; ++b) {
      s0[b] = b <= dq ? s0[b] : -1e30f; s1[b] = b + 8 <= dq ? s1[b] : -1e30f;
      mx = fmaxf(mx, fmaxf(s0[b], s1[b]));
    }
    mx = xmax32(xmax16(mx));
    const float mc = mx * SCL2;
    float e[8];
    const float em0 = __builtin_amdgcn_exp2f(sm[0] * SCL2 - mc), em1 = __builtin_amdgcn_exp2f(sm[1] * SCL2 - mc);
    float lx = em0 + em1;
#pragma unroll
    for (int b = 0; b < 4; ++b) {
      e[b] = __builtin_amdgcn_exp2f(s0[b] * SCL2 - mc); e[4 + b] = __builtin_amdgcn_exp2f(s1[b] * SCL2 - mc);
      lx += e[b] + e[4 + b];
    }
    u4 pu = {pk2(e[0], e[1]), pk2(e[2], e[3]), pk2(e[4], e[5]), pk2(e[6], e[7])};
    u2 pm = {pk2(em0, em1), pk2(em0, em1)};
    const float inv = __builtin_amdgcn_rcpf(xadd16(xadd32(lx)));
    asm volatile("s_nop 1" : "+v"(pu), "+v"(pm));
    f4 o[8];
#pragma unroll
    for (int et = 0; et < 8; ++et) o[et] = amf32z(vx[et], __builtin_bit_cast(bf8, pu));
#pragma unroll
    for (int et = 0; et < 8; ++et) amf16(o[et], (et & 1) ? u2{vm[et >> 1].z, vm[et >> 1].w} : u2{vm[et >> 1].x, vm[et >> 1].y}, pm);
    pin<7>(o[6], o[7]);
    const unsigned ow = aOW + ob;
#pragma unroll
    for (int et = 0; et < 8; ++et) {
      asm volatile("" : "+v"(o[et]));
      *(LAS u2*)(L + ow + et * 1024) = u2{pk2(o[et][0] * inv, o[et][1] * inv), pk2(o[et][2] * inv, o[et][3] * inv)};
    }
    bar();
    {
      const unsigned orr = l16 + ob;
      f16v c5;
      bf8 ob8[16];
#pragma unroll
      for (int k = 0; k < 16; ++k) ob8[k] = rd16(L, orr, k * 1024);
      unsigned fl = look(s + sd, nr);
      SB();
      const unsigned sp = OFF_PART + (s * NPART + 8 + hp) * PART_SLOT + cb0 * 2048;
      const int bm = s < 63 ? 31 : (cp == 3 ? 24 : 1 << cp);
      asm volatile("" : "=v"(acc[0]), "=v"(acc[1]), "=v"(acc[2]), "=v"(acc[3]), "=v"(c5));
      if (bm & 1) oproj1<0, 0>(acc[0], ob8);
      if (bm & 2) oproj1<64, 0>(acc[1], ob8);
      if (bm & 4) oproj1<128, 0>(acc[2], ob8);
      if (bm & 8) oproj1<192, 0>(acc[3], ob8);
      if ((bm & 16) && w < 2) {
        asm volatile("v_mfma_f32_32x32x16_bf16 %0, %1, %2, 0" : "=&v"(c5) : "v"(Wo4[0]), "v"(ob8[0]));
#pragma unroll
        for (int k = 1; k < 16; ++k) asm volatile("v_mfma_f32_32x32x16_bf16 %0, %1, %2, %0" : "+v"(c5) : "v"(Wo4[k]), "v"(ob8[k]));
      }
      asm volatile("s_nop 7\n\ts_nop 3" : "+v"(acc[0]), "+v"(acc[1]), "+v"(acc[2]), "+v"(acc[3]), "+v"(c5));
      miss = __ballot(fl == ~0u);
      asm volatile("" : "+s"(miss)::"memory");
      SB();
#pragma unroll
      for (int i = 0; i < 4; ++i)
        if (bm & (1 << i)) { st16(pk8(acc[i], 0), rws, l16, sp + i * 2048); st16(pk8(acc[i], 1), rws, l16 + 1024, sp + i * 2048); }
      if ((bm & 16) && w < 2) { st16(pk8(c5, 0), rws, l16, sp + 8192); st16(pk8(c5, 1), rws, l16 + 1024, sp + 8192); }
    }
    ob ^= 16384;
    WST1(wid + 64, 65 + (near ? s & ~3 : ((s - NSEG + (c < 3 ? 1 : 0)) >> 3) * 4));
  }
  if (wid == 128 && w == 0) {
    for (int sp = 0; sp < SPIN_MAX; ++sp) {
      const unsigned v = max(max3u(ld4(rws, l * 64, OFF_FM), ld4(rws, l * 64, OFF_FP), ld4(rws, l * 64, OFF_FP + 4096)), max(ld4(rws, l * 64, OFF_FP + 8192), ld4(rws, l * 64, OFF_FP + 12288)));
      if (__ballot(v == ~0u) == 0ull) break;
      asm volatile("" ::: "memory");
    }
    st8(call_tag(rws), rws, vfl, OFF_GEN);
  }
}

__global__ void __launch_bounds__(256, 1) main_kernel(Prm p) {
  __shared__ __attribute__((aligned(16))) char smem[163840];
  LAS char* L = (LAS char*)smem;
  const int x = blockIdx.x & 7, j = blockIdx.x >> 3;
  if (x < 4 && j < 16) {
    role_r(p, L, x * 16 + j);
    __builtin_amdgcn_endpgm();
  }
  role_w(p, L, x < 4 ? 112 + x * 16 + j : (x - 4) * 32 + j);
}

#define D1 16
#define D3 96
#define D4 0
#define D2 58
static Prm make_prm(void* const* d_in, void* d_out, void* d_ws) {
  Prm p;
  p.x = (const u16*)d_in[0]; p.mem0 = (const u16*)d_in[1]; p.Wq = (const u16*)d_in[2]; p.Wk = (const u16*)d_in[3]; p.Wv = (const u16*)d_in[4]; p.Wo = (const u16*)d_in[5];
  p.wg = (const u16*)d_in[8]; p.wu = (const u16*)d_in[9]; p.wd = (const u16*)d_in[10];
  p.ws = (unsigned char*)(((uintptr_t)d_ws + 0x1FFFFF) & ~(uintptr_t)0x1FFFFF); p.out = (u16*)d_out; p.d1 = D1; p.d2 = D2; p.d3 = D3; p.d4 = D4; p.mode = 0; p.pad = 0;
  return p;
}
extern "C" void kernel_launch(void* const* d_in, const int* in_sizes, int n_in, void* d_out, int out_size, void* d_ws, size_t ws_size, hipStream_t stream) {
  (void)in_sizes; (void)n_in; (void)out_size; (void)ws_size;
  Prm p = make_prm(d_in, d_out, d_ws);
  hipLaunchKernelGGL(main_kernel, dim3(256), dim3(256), 0, stream, p);
}
```
